```python
import torch

def _softmax(x, axis=-1):
    return torch.softmax(x, dim=axis)

def _gelu(x):
    return torch.nn.functional.gelu(x, approximate='tanh')

def _silu(x):
    return torch.nn.functional.silu(x)

def _sigmoid(x):
    return torch.sigmoid(x)

def _layernorm(x, axis=-1, eps=1e-05):
    m = x.mean(dim=axis, keepdim=True)
    v = x.var(dim=axis, keepdim=True, unbiased=False)
    return (x - m) / torch.sqrt(v + eps)

def _scatter_add(tgt, idx, src, axis=0):
    return tgt.clone().index_add_(axis, idx, src)

def _segment_sum(src, seg, n):
    out = torch.zeros((n,) + src.shape[1:], dtype=src.dtype)
    return out.index_add_(0, seg, src)

def _pad(x, pw):
    flat = tuple((v for pair in reversed(pw) for v in pair))
    return torch.nn.functional.pad(x, flat)
kernel_name = 'standalone__swin_window_attn__vision'

def setup_inputs():

    _w = lambda a: (lambda t: t.to(torch.bfloat16) if t.is_floating_point() else t)(torch.as_tensor(a))
    H = W = 224 // 7 * 7
    C, win = (192, 7)
    return {'x': _w((lambda *s: torch.randn(*s, dtype=torch.bfloat16) * 0.2)(1, H, W, C)), 'wqkv': _w((lambda *s: torch.randn(*s, dtype=torch.bfloat16) * 0.2)(C, 3 * C)), 'wo': _w((lambda *s: torch.randn(*s, dtype=torch.bfloat16) * 0.05)(C, C))}

def reference(x, wqkv, wo):
    B, H, W, C = x.shape
    win = 7
    nh, nw = (H // win, W // win)
    xw = (lambda x, *d: x.permute(*d))(x.reshape(B, nh, win, nw, win, C), 0, 1, 3, 2, 4, 5)
    xw = xw.reshape(B * nh * nw, win * win, C)
    qkv = xw @ wqkv
    q, k, v = (qkv[..., :C].clone(), qkv[..., C:2 * C].clone(), qkv[..., 2 * C:].clone())
    scale = C ** (-0.5)
    p = _softmax(torch.einsum('bqc,bkc->bqk', q, k) * scale, axis=-1)
    o = torch.einsum('bqk,bkc->bqc', p, v) @ wo
    o = (lambda x, *d: x.permute(*d))(o.reshape(B, nh, nw, win, win, C), 0, 1, 3, 2, 4, 5)
    return o.reshape(B, H, W, C)
```

```cpp
#include <hip/hip_runtime.h>
#include <hip/hip_bf16.h>

typedef __bf16 bf16x8 __attribute__((ext_vector_type(8)));
typedef __bf16 bf16x2 __attribute__((ext_vector_type(2)));
typedef float f32x4 __attribute__((ext_vector_type(4)));
typedef float f32x2 __attribute__((ext_vector_type(2)));
typedef unsigned u32x4 __attribute__((ext_vector_type(4)));
typedef unsigned u32x2 __attribute__((ext_vector_type(2)));

#define DI __device__ __forceinline__
#define MFMA(a, b, c) __builtin_amdgcn_mfma_f32_16x16x32_bf16((a), (b), (c), 0, 0, 0)

constexpr int C = 192;
constexpr int IMG = 224;
constexpr int FRAG_BYTES = 1024;
constexpr int MAT_FRAGS = 72;
constexpr int MAT_BYTES = MAT_FRAGS * FRAG_BYTES;

DI unsigned cvtpk(float lo, float hi) {
  f32x2 v = {lo, hi};
  bf16x2 b = __builtin_convertvector(v, bf16x2);
  return __builtin_bit_cast(unsigned, b);
}
DI bf16x8 pack8(const f32x4 a, const f32x4 b) {
  u32x4 p = {cvtpk(a[0], a[1]), cvtpk(a[2], a[3]), cvtpk(b[0], b[1]), cvtpk(b[2], b[3])};
  return __builtin_bit_cast(bf16x8, p);
}
DI int chan(int ct, int i) { return 32 * (ct >> 1) + 8 * (i >> 2) + 4 * (ct & 1) + (i & 3); }

__global__ __launch_bounds__(64) void fold_kernel(const unsigned short* __restrict__ wqkv,
                                                  const unsigned short* __restrict__ wo,
                                                  u32x4* __restrict__ wf) {
  const int f = blockIdx.x;
  const int mat = f / MAT_FRAGS;
  const int r = f - mat * MAT_FRAGS;
  const int ct = r / 6, s = r - ct * 6;
  const int lane = threadIdx.x, i = lane & 15, g = lane >> 4;
  const int k0 = chan(2 * s, i), k1 = chan(2 * s + 1, i);
  const int cn = chan(ct, i);
  const int aoff = mat == 0 ? 0 : 2 * C;
  bf16x8 a0[6], a1[6], b[6];
#pragma unroll
  for (int u = 0; u < 6; ++u) {
    a0[u] = *(const bf16x8*)(wqkv + k0 * (3 * C) + aoff + 32 * u + 8 * g);
    a1[u] = *(const bf16x8*)(wqkv + k1 * (3 * C) + aoff + 32 * u + 8 * g);
  }
  if (mat == 0) {
#pragma unroll
    for (int u = 0; u < 6; ++u) b[u] = *(const bf16x8*)(wqkv + cn * (3 * C) + C + 32 * u + 8 * g);
  } else {
#pragma unroll
    for (int u = 0; u < 6; ++u) {
      u32x4 t;
#pragma unroll
      for (int j = 0; j < 4; ++j) {
        unsigned lo = wo[(32 * u + 8 * g + 2 * j) * C + cn];
        unsigned hi = wo[(32 * u + 8 * g + 2 * j + 1) * C + cn];
        t[j] = lo | (hi << 16);
      }
      b[u] = __builtin_bit_cast(bf16x8, t);
    }
  }
  f32x4 d0 = {0.f, 0.f, 0.f, 0.f}, d1 = {0.f, 0.f, 0.f, 0.f};
#pragma unroll
  for (int u = 0; u < 6; ++u) {
    d0 = MFMA(a0[u], b[u], d0);
    d1 = MFMA(a1[u], b[u], d1);
  }
  const float sc = mat == 0 ? 0.07216878364870322f * 1.4426950408889634f : 1.0f;
  d0 *= sc;
  d1 *= sc;
  wf[f * 64 + lane] = __builtin_bit_cast(u32x4, pack8(d0, d1));
}

DI float xmax4(float m) {
  u32x2 t = __builtin_amdgcn_permlane32_swap(__float_as_uint(m), __float_as_uint(m), false, false);
  m = fmaxf(__uint_as_float(t[0]), __uint_as_float(t[1]));
  t = __builtin_amdgcn_permlane16_swap(__float_as_uint(m), __float_as_uint(m), false, false);
  return fmaxf(__uint_as_float(t[0]), __uint_as_float(t[1]));
}
DI float xsum4(float m) {
  u32x2 t = __builtin_amdgcn_permlane32_swap(__float_as_uint(m), __float_as_uint(m), false, false);
  m = __uint_as_float(t[0]) + __uint_as_float(t[1]);
  t = __builtin_amdgcn_permlane16_swap(__float_as_uint(m), __float_as_uint(m), false, false);
  return __uint_as_float(t[0]) + __uint_as_float(t[1]);
}

__global__ __launch_bounds__(256, 1) void swin_main(const void* __restrict__ x, const u32x4* __restrict__ wf,
                                                    void* __restrict__ out) {
  extern __shared__ __attribute__((aligned(16))) char smem[];
  const int tid = threadIdx.x, lane = tid & 63;
  const int wid = __builtin_amdgcn_readfirstlane(tid >> 6);
  const int i16 = lane & 15, g = lane >> 4;
  const int win = blockIdx.x * 4 + wid;
  const int wy = win >> 5, wx = win & 31;
  constexpr unsigned XBYTES = (unsigned)IMG * IMG * C * 2;
  const __amdgpu_buffer_rsrc_t xr = __builtin_amdgcn_make_buffer_rsrc((void*)x, 0, XBYTES, 0x00020000);
  const __amdgpu_buffer_rsrc_t orr = __builtin_amdgcn_make_buffer_rsrc(out, 0, XBYTES, 0x00020000);

  unsigned off[4];
#pragma unroll
  for (int tt = 0; tt < 4; ++tt) {
    const int t = 16 * tt + i16;
    const int ti = (t * 37) >> 8;
    const int tj = t - 7 * ti;
    const unsigned o = (unsigned)(((7 * wy + ti) * IMG + 7 * wx + tj) * (C * 2) + 16 * g);
    off[tt] = t < 49 ? o : 0x80000000u;
  }
  bf16x8 xf[4][6];
#pragma unroll
  for (int s = 0; s < 6; ++s)
#pragma unroll
    for (int tt = 0; tt < 4; ++tt)
      xf[tt][s] = __builtin_bit_cast(bf16x8, __builtin_amdgcn_raw_buffer_load_b128(xr, off[tt] + 64 * s, 0, 0));

#pragma unroll
  for (int it = 0; it < 36; ++it) {
    const int f = it * 4 + wid;
    __builtin_amdgcn_global_load_lds((const void*)(wf + f * 64 + lane), (__attribute__((address_space(3))) void*)(smem + f * FRAG_BYTES), 16, 0, 0);
  }
  asm volatile("s_waitcnt vmcnt(0)" ::: "memory");
  __syncthreads();

  const bf16x8* lm = (const bf16x8*)smem + lane;
  const bf16x8* lv = (const bf16x8*)(smem + MAT_BYTES) + lane;
  const f32x4 zero = {0.f, 0.f, 0.f, 0.f};

  f32x4 S[4][4];
#pragma unroll
  for (int kt = 0; kt < 4; ++kt)
#pragma unroll
    for (int qt = 0; qt < 4; ++qt) S[kt][qt] = zero;
#pragma unroll
  for (int cp = 0; cp < 6; ++cp) {
    f32x4 q0[4], q1[4];
#pragma unroll
    for (int qt = 0; qt < 4; ++qt) { q0[qt] = zero; q1[qt] = zero; }
#pragma unroll
    for (int s = 0; s < 6; ++s) {
      const bf16x8 a0 = lm[((2 * cp) * 6 + s) * 64];
      const bf16x8 a1 = lm[((2 * cp + 1) * 6 + s) * 64];
#pragma unroll
      for (int qt = 0; qt < 4; ++qt) {
        q0[qt] = MFMA(a0, xf[qt][s], q0[qt]);
        q1[qt] = MFMA(a1, xf[qt][s], q1[qt]);
      }
    }
#pragma unroll
    for (int qt = 0; qt < 4; ++qt) {
      const bf16x8 qb = pack8(q0[qt], q1[qt]);
#pragma unroll
      for (int kt = 0; kt < 4; ++kt) S[kt][qt] = MFMA(xf[kt][cp], qb, S[kt][qt]);
    }
  }

  bf16x8 pf[2][4];
  const bool g0 = (g == 0);
#pragma unroll
  for (int qt = 0; qt < 4; ++qt) {
    const float s48 = g0 ? S[3][qt][0] : -INFINITY;
    float m = s48;
#pragma unroll
    for (int kt = 0; kt < 3; ++kt)
#pragma unroll
      for (int r = 0; r < 4; ++r) m = fmaxf(m, S[kt][qt][r]);
    m = xmax4(m);
    float p[3][4];
    float p48 = __builtin_amdgcn_exp2f(s48 - m);
    float sum = p48;
#pragma unroll
    for (int kt = 0; kt < 3; ++kt)
#pragma unroll
      for (int r = 0; r < 4; ++r) {
        p[kt][r] = __builtin_amdgcn_exp2f(S[kt][qt][r] - m);
        sum += p[kt][r];
      }
    sum = xsum4(sum);
    const float inv = __builtin_amdgcn_rcpf(sum);
    u32x4 w0 = {cvtpk(p[0][0] * inv, p[0][1] * inv), cvtpk(p[0][2] * inv, p[0][3] * inv),
                cvtpk(p[1][0] * inv, p[1][1] * inv), cvtpk(p[1][2] * inv, p[1][3] * inv)};
    u32x4 w1 = {cvtpk(p[2][0] * inv, p[2][1] * inv), cvtpk(p[2][2] * inv, p[2][3] * inv),
                cvtpk(p48 * inv, 0.f), 0u};
    pf[0][qt] = __builtin_bit_cast(bf16x8, w0);
    pf[1][qt] = __builtin_bit_cast(bf16x8, w1);
  }

#pragma unroll
  for (int cp = 0; cp < 6; ++cp) {
    f32x4 o[2][4];
#pragma unroll
    for (int h = 0; h < 2; ++h) {
      f32x4 v[4];
#pragma unroll
      for (int tt = 0; tt < 4; ++tt) v[tt] = zero;
#pragma unroll
      for (int s = 0; s < 6; ++s) {
        const bf16x8 b = lv[((2 * cp + h) * 6 + s) * 64];
#pragma unroll
        for (int tt = 0; tt < 4; ++tt) v[tt] = MFMA(xf[tt][s], b, v[tt]);
      }
      const bf16x8 va0 = pack8(v[0], v[1]);
      const bf16x8 va1 = pack8(v[2], v[3]);
#pragma unroll
      for (int qt = 0; qt < 4; ++qt) {
        o[h][qt] = MFMA(va0, pf[0][qt], zero);
        o[h][qt] = MFMA(va1, pf[1][qt], o[h][qt]);
      }
    }
#pragma unroll
    for (int qt = 0; qt < 4; ++qt)
      __builtin_amdgcn_raw_buffer_store_b128(__builtin_bit_cast(u32x4, pack8(o[0][qt], o[1][qt])), orr,
                                             off[qt] + 64 * cp, 0, 0);
  }
}

extern "C" void kernel_launch(void* const* d_in, const int* in_sizes, int n_in, void* d_out, int out_size,
                              void* d_ws, size_t ws_size, hipStream_t stream) {
  const unsigned short* wqkv = (const unsigned short*)d_in[1];
  const unsigned short* wo = (const unsigned short*)d_in[2];
  u32x4* wf = (u32x4*)d_ws;
  fold_kernel<<<2 * MAT_FRAGS, 64, 0, stream>>>(wqkv, wo, wf);
  (void)hipFuncSetAttribute((const void*)swin_main, hipFuncAttributeMaxDynamicSharedMemorySize, 2 * MAT_BYTES);
  swin_main<<<256, 256, 2 * MAT_BYTES, stream>>>(d_in[0], wf, d_out);
}
```

```cpp
#include <hip/hip_runtime.h>

constexpr unsigned LDS_BYTES = 156224;


__global__ __launch_bounds__(256, 1) __attribute__((aligned(4096))) void swin_main(const void* __restrict__ x, const void* __restrict__ wqkv,
                                                    const void* __restrict__ wo, void* __restrict__ ws,
                                                    void* __restrict__ out) {
  unsigned tid = threadIdx.x;
  const unsigned bid = blockIdx.x;
  unsigned long long karg = (unsigned long long)__builtin_amdgcn_kernarg_segment_ptr();
  asm volatile(
    "s_load_dwordx2 s[36:37], s[0:1], 8\n\t"
    "s_load_dwordx2 s[8:9], s[0:1], 0\n\t"
    "s_load_dwordx2 s[16:17], s[0:1], 24\n\t"
    "s_load_dwordx2 s[40:41], s[0:1], 16\n\t"
    "s_load_dwordx2 s[12:13], s[0:1], 32\n\t"
    "s_mov_b32 s2, 0\n\t"
    "s_cmp_lt_u32 s34, 216\n\t"
    "s_cbranch_scc1 Lnopf%=\n\t"
    "s_getpc_b64 s[24:25]\n\t"
    "Lpc%=:\n\t"
    "v_lshlrev_b32 v15, 7, v0\n\t"
    "v_min_u32 v15, Lend%=-Lpc%=-4, v15\n\t"
    "global_load_dword v36, v15, s[24:25]\n\t"
    "Lnopf%=:\n\t"
    "v_lshrrev_b32 v41, 6, v0\n\t"
    "v_and_b32 v40, 63, v0\n\t"
    "s_getreg_b32 s3, hwreg(HW_REG_XCC_ID)\n\t"
    "v_readfirstlane_b32 s35, v41\n\t"
    "s_andn2_b32 s3, 4, s3\n\t"
    "s_cmp_lt_u32 s34, 144\n\t"
    "s_cselect_b32 s3, s3, 0\n\t"
    "s_sub_u32 s31, s34, 144\n\t"
    "s_cmp_lt_u32 s31, 72\n\t"
    "s_cselect_b32 s31, 1, s3\n\t"
    "Lrestart%=:\n\t"
    "s_lshr_b32 s53, s34, 3\n\t"
    "s_and_b32 s59, s34, 7\n\t"
    "v_lshrrev_b32 v42, 3, v40\n\t"
    "v_and_b32 v43, 6, v42\n\t"
    "v_and_b32 v44, 7, v40\n\t"
    "v_xor_b32 v43, v43, v44\n\t"
    "v_lshlrev_b32 v43, 4, v43\n\t"
    "s_cmp_eq_u32 s35, 3\n\t"
    "s_cselect_b32 s26, s31, s2\n\t"
    "s_cmp_eq_u32 s26, 0\n\t"
    "s_cbranch_scc1 Lother%=\n\t"
    "s_mov_b32 s38, 221184\n\t"
    "s_mov_b32 s39, 0x20000\n\t"
    "v_lshrrev_b32 v45, 2, v42\n\t"
    "v_and_b32 v46, 3, v42\n\t"
    "v_lshl_add_u32 v45, v45, 3, v46\n\t"
    "v_mul_u32_u24 v45, 1152, v45\n\t"
    "v_add_u32 v45, v45, v43\n\t"
    "s_and_b32 s26, s59, 3\n\t"
    "s_lshl_b32 s61, s53, 2\n\t"
    "s_add_u32 s61, s61, s26\n\t"
    "s_mov_b32 s54, 1\n\t"
    "s_mov_b32 s55, 0\n\t"
    "s_cmp_ge_u32 s34, 144\n\t"
    "s_cbranch_scc1 LWpre%=\n\t"
    "Ldec%=:\n\t"
    "s_mul_i32 s62, s61, 43\n\t"
    "s_lshr_b32 s62, s62, 8\n\t"
    "s_mul_i32 s26, s62, 6\n\t"
    "s_sub_u32 s63, s61, s26\n\t"
    "s_mul_i32 s26, s63, 36864\n\t"
    "s_add_u32 s55, s55, s26\n\t"
    "s_lshr_b32 s26, s62, 1\n\t"
    "s_lshl_b32 s26, s26, 3\n\t"
    "s_and_b32 s56, s62, 1\n\t"
    "s_add_u32 s56, s56, s26\n\t"
    "s_mul_i32 s56, s56, 4608\n\t"
    "s_add_u32 s56, s56, 384\n\t"
    "s_add_u32 s27, s55, 18432\n\t"
    "s_add_u32 s28, s55, 4608\n\t"
    "s_add_u32 s29, s55, 23040\n\t"
    "s_add_u32 s57, s56, 18432\n\t"
    "s_mov_b32 m0, 86016\n\t"
    "s_cmp_eq_u32 s54, 2\n\t"
    "s_waitcnt lgkmcnt(0)\n\t"
    "buffer_load_dwordx4 v45, s[36:39], s55 offen offset:0 lds\n\t"
    "s_mov_b32 m0, 89088\n\t"
    "s_nop 0\n\t"
    "buffer_load_dwordx4 v45, s[36:39], s27 offen offset:0 lds\n\t"
    "s_mov_b32 m0, 104448\n\t"
    "s_nop 0\n\t"
    "buffer_load_dwordx4 v45, s[36:39], s28 offen offset:0 lds\n\t"
    "s_mov_b32 m0, 107520\n\t"
    "s_nop 0\n\t"
    "buffer_load_dwordx4 v45, s[36:39], s29 offen offset:0 lds\n\t"
    "s_mov_b32 m0, 122880\n\t"
    "s_cbranch_scc1 LnoB0%=\n\t"
    "buffer_load_dwordx4 v45, s[36:39], s56 offen offset:0 lds\n\t"
    "s_mov_b32 m0, 125952\n\t"
    "s_nop 0\n\t"
    "buffer_load_dwordx4 v45, s[36:39], s57 offen offset:0 lds\n\t"
    "LnoB0%=:\n\t"
    "s_mov_b32 m0, 86912\n\t"
    "s_nop 0\n\t"
    "buffer_load_dwordx4 v45, s[36:39], s55 offen offset:128 lds\n\t"
    "s_mov_b32 m0, 89984\n\t"
    "s_nop 0\n\t"
    "buffer_load_dwordx4 v45, s[36:39], s27 offen offset:128 lds\n\t"
    "s_mov_b32 m0, 105344\n\t"
    "s_nop 0\n\t"
    "buffer_load_dwordx4 v45, s[36:39], s28 offen offset:128 lds\n\t"
    "s_mov_b32 m0, 108416\n\t"
    "s_nop 0\n\t"
    "buffer_load_dwordx4 v45, s[36:39], s29 offen offset:128 lds\n\t"
    "s_mov_b32 m0, 123776\n\t"
    "s_cbranch_scc1 LnoB1%=\n\t"
    "buffer_load_dwordx4 v45, s[36:39], s56 offen offset:128 lds\n\t"
    "s_mov_b32 m0, 126848\n\t"
    "s_nop 0\n\t"
    "buffer_load_dwordx4 v45, s[36:39], s57 offen offset:128 lds\n\t"
    "LnoB1%=:\n\t"
    "s_mov_b32 m0, 87808\n\t"
    "s_nop 0\n\t"
    "buffer_load_dwordx4 v45, s[36:39], s55 offen offset:256 lds\n\t"
    "s_mov_b32 m0, 90880\n\t"
    "s_nop 0\n\t"
    "buffer_load_dwordx4 v45, s[36:39], s27 offen offset:256 lds\n\t"
    "s_mov_b32 m0, 106240\n\t"
    "s_nop 0\n\t"
    "buffer_load_dwordx4 v45, s[36:39], s28 offen offset:256 lds\n\t"
    "s_mov_b32 m0, 109312\n\t"
    "s_nop 0\n\t"
    "buffer_load_dwordx4 v45, s[36:39], s29 offen offset:256 lds\n\t"
    "s_mov_b32 m0, 124672\n\t"
    "s_cbranch_scc1 LnoB2%=\n\t"
    "buffer_load_dwordx4 v45, s[36:39], s56 offen offset:256 lds\n\t"
    "s_mov_b32 m0, 127744\n\t"
    "s_nop 0\n\t"
    "buffer_load_dwordx4 v45, s[36:39], s57 offen offset:256 lds\n\t"
    "LnoB2%=:\n\t"
    "s_cmp_lg_u32 s2, 0\n\t"
    "s_cbranch_scc1 Lnba%=\n\t"
    "s_barrier\n\t"
    "s_branch Lnba%=\n\t"
    "Lother%=:\n\t"
    "s_mov_b32 s10, 19267584\n\t"
    "s_mov_b32 s11, 0x20000\n\t"
    "s_mov_b32 s14, 19267584\n\t"
    "s_mov_b32 s15, 0x20000\n\t"
    "s_mov_b32 s18, 210944\n\t"
    "s_mov_b32 s19, 0x20000\n\t"
    "s_add_u32 s26, s35, 1\n\t"
    "s_and_b32 s26, s26, 2\n\t"
    "s_cbranch_scc1 Lld%=\n\t"
    "s_waitcnt lgkmcnt(0)\n\t"
    "s_memrealtime s[22:23]\n\t"
    "s_load_dwordx2 s[44:45], s[16:17], 158720\n\t"
    "s_cmp_eq_u32 s31, 0\n\t"
    "s_cbranch_scc1 Lnia%=\n\t"
    "s_barrier\n\t"
    "s_branch Lnia%=\n\t"
    "Lld%=:\n\t"
    "s_mul_i32 s48, s53, 602112\n\t"
    "s_mul_i32 s26, s59, 10752\n\t"
    "s_add_u32 s48, s48, s26\n\t"
    "s_mov_b32 s52, 83328\n\t"
    "v_mul_u32_u24 v37, 37, v42\n\t"
    "v_mul_u32_u24 v38, 384, v42\n\t"
    "v_add_u32 v38, v38, v43\n\t"
    "v_add_u32 v39, 0, v37\n\t"
    "v_lshrrev_b32 v39, 8, v39\n\t"
    "v_mad_u32_u24 v0, v39, s52, v38\n\t"
    "v_add_u32 v39, 296, v37\n\t"
    "v_lshrrev_b32 v39, 8, v39\n\t"
    "v_mad_u32_u24 v1, v39, s52, v38\n\t"
    "v_add_u32 v39, 592, v37\n\t"
    "v_lshrrev_b32 v39, 8, v39\n\t"
    "v_mad_u32_u24 v2, v39, s52, v38\n\t"
    "v_add_u32 v39, 888, v37\n\t"
    "v_lshrrev_b32 v39, 8, v39\n\t"
    "v_mad_u32_u24 v3, v39, s52, v38\n\t"
    "s_mul_i32 s63, s35, 5376\n\t"
    "s_add_u32 s63, s63, s48\n\t"
    "s_sub_u32 s63, s63, 5376\n\t"
    "s_mul_i32 s62, s35, 36864\n\t"
    "s_add_u32 s62, s62, 36864\n\t"
    "s_waitcnt lgkmcnt(0)\n\t"
    "s_load_dwordx2 s[44:45], s[16:17], 158720\n\t"
    "s_cmp_eq_u32 s31, 0\n\t"
    "s_cbranch_scc1 Lng0%=\n\t"
    "s_barrier\n\t"
    "Lng0%=:\n\t"
    "s_add_u32 m0, s62, 0\n\t"
    "s_add_u32 s26, s63, 0\n\t"
    "buffer_load_dwordx4 v0, s[8:11], s26 offen offset:0 lds\n\t"
    "s_add_u32 m0, s62, 896\n\t"
    "s_add_u32 s26, s63, 0\n\t"
    "buffer_load_dwordx4 v0, s[8:11], s26 offen offset:128 lds\n\t"
    "s_add_u32 m0, s62, 1792\n\t"
    "s_add_u32 s26, s63, 0\n\t"
    "buffer_load_dwordx4 v0, s[8:11], s26 offen offset:256 lds\n\t"
    "s_add_u32 m0, s62, 18432\n\t"
    "s_add_u32 s26, s63, 2688\n\t"
    "buffer_load_dwordx4 v0, s[8:11], s26 offen offset:0 lds\n\t"
    "s_add_u32 m0, s62, 19328\n\t"
    "s_add_u32 s26, s63, 2688\n\t"
    "buffer_load_dwordx4 v0, s[8:11], s26 offen offset:128 lds\n\t"
    "s_add_u32 m0, s62, 20224\n\t"
    "s_add_u32 s26, s63, 2688\n\t"
    "buffer_load_dwordx4 v0, s[8:11], s26 offen offset:256 lds\n\t"
    "s_add_u32 m0, s62, 3072\n\t"
    "s_add_u32 s26, s63, 3072\n\t"
    "buffer_load_dwordx4 v1, s[8:11], s26 offen offset:0 lds\n\t"
    "s_cmp_eq_u32 s3, 0\n\t"
    "s_cbranch_scc1 Lng7%=\n\t"
    "s_barrier\n\t"
    "Lng7%=:\n\t"
    "s_add_u32 m0, s62, 3968\n\t"
    "s_add_u32 s26, s63, 3072\n\t"
    "buffer_load_dwordx4 v1, s[8:11], s26 offen offset:128 lds\n\t"
    "s_add_u32 m0, s62, 4864\n\t"
    "s_add_u32 s26, s63, 3072\n\t"
    "buffer_load_dwordx4 v1, s[8:11], s26 offen offset:256 lds\n\t"
    "s_add_u32 m0, s62, 21504\n\t"
    "s_add_u32 s26, s63, 5760\n\t"
    "buffer_load_dwordx4 v1, s[8:11], s26 offen offset:0 lds\n\t"
    "s_add_u32 m0, s62, 22400\n\t"
    "s_add_u32 s26, s63, 5760\n\t"
    "buffer_load_dwordx4 v1, s[8:11], s26 offen offset:128 lds\n\t"
    "s_add_u32 m0, s62, 23296\n\t"
    "s_add_u32 s26, s63, 5760\n\t"
    "buffer_load_dwordx4 v1, s[8:11], s26 offen offset:256 lds\n\t"
    "s_waitcnt vmcnt(11)\n\t"
    "s_add_u32 m0, s62, 6144\n\t"
    "s_add_u32 s26, s63, 6144\n\t"
    "buffer_load_dwordx4 v2, s[8:11], s26 offen offset:0 lds\n\t"
    "s_waitcnt vmcnt(11)\n\t"
    "s_add_u32 m0, s62, 7040\n\t"
    "s_add_u32 s26, s63, 6144\n\t"
    "buffer_load_dwordx4 v2, s[8:11], s26 offen offset:128 lds\n\t"
    "s_waitcnt vmcnt(11)\n\t"
    "s_add_u32 m0, s62, 7936\n\t"
    "s_add_u32 s26, s63, 6144\n\t"
    "buffer_load_dwordx4 v2, s[8:11], s26 offen offset:256 lds\n\t"
    "s_waitcnt vmcnt(11)\n\t"
    "s_add_u32 m0, s62, 24576\n\t"
    "s_add_u32 s26, s63, 8832\n\t"
    "buffer_load_dwordx4 v2, s[8:11], s26 offen offset:0 lds\n\t"
    "s_waitcnt vmcnt(11)\n\t"
    "s_add_u32 m0, s62, 25472\n\t"
    "s_add_u32 s26, s63, 8832\n\t"
    "buffer_load_dwordx4 v2, s[8:11], s26 offen offset:128 lds\n\t"
    "s_waitcnt vmcnt(11)\n\t"
    "s_add_u32 m0, s62, 26368\n\t"
    "s_add_u32 s26, s63, 8832\n\t"
    "buffer_load_dwordx4 v2, s[8:11], s26 offen offset:256 lds\n\t"
    "s_waitcnt vmcnt(11)\n\t"
    "s_add_u32 m0, s62, 9216\n\t"
    "s_add_u32 s26, s63, 9216\n\t"
    "buffer_load_dwordx4 v3, s[8:11], s26 offen offset:0 lds\n\t"
    "s_waitcnt vmcnt(11)\n\t"
    "s_add_u32 m0, s62, 10112\n\t"
    "s_add_u32 s26, s63, 9216\n\t"
    "buffer_load_dwordx4 v3, s[8:11], s26 offen offset:128 lds\n\t"
    "s_waitcnt vmcnt(11)\n\t"
    "s_add_u32 m0, s62, 11008\n\t"
    "s_add_u32 s26, s63, 9216\n\t"
    "buffer_load_dwordx4 v3, s[8:11], s26 offen offset:256 lds\n\t"
    "s_waitcnt vmcnt(11)\n\t"
    "s_add_u32 m0, s62, 27648\n\t"
    "s_add_u32 s26, s63, 11904\n\t"
    "buffer_load_dwordx4 v3, s[8:11], s26 offen offset:0 lds\n\t"
    "s_waitcnt vmcnt(11)\n\t"
    "s_add_u32 m0, s62, 28544\n\t"
    "s_add_u32 s26, s63, 11904\n\t"
    "buffer_load_dwordx4 v3, s[8:11], s26 offen offset:128 lds\n\t"
    "s_waitcnt vmcnt(11)\n\t"
    "s_add_u32 m0, s62, 29440\n\t"
    "s_add_u32 s26, s63, 11904\n\t"
    "buffer_load_dwordx4 v3, s[8:11], s26 offen offset:256 lds\n\t"
    "v_and_b32 v50, 15, v40\n\t"
    "v_lshrrev_b32 v51, 4, v40\n\t"
    "v_lshrrev_b32 v55, 3, v50\n\t"
    "v_mul_u32_u24 v55, 3072, v55\n\t"
    "v_and_b32 v56, 7, v50\n\t"
    "v_lshl_add_u32 v55, v56, 7, v55\n\t"
    "v_and_b32 v56, 6, v50\n\t"
    "v_xor_b32 v56, v56, v51\n\t"
    "v_lshl_add_u32 v55, v56, 4, v55\n\t"
    "v_xor_b32 v56, 64, v55\n\t"
    "v_mov_b32 v54, 2147483648\n\t"
    "v_lshlrev_b32 v7, 4, v40\n\t"
    "s_waitcnt lgkmcnt(0)\n\t"
    "s_mul_i32 s44, s44, 0x41c64e6d\n\t"
    "s_add_u32 s44, s44, 0x3039\n\t"
    "s_mul_i32 s45, s45, 0x915f77f5\n\t"
    "s_add_u32 s45, s45, 0x9e3779b9\n\t"
    "s_xor_b32 s45, s45, s44\n\t"
    "v_mov_b32 v63, v54\n\t"
    "s_branch Lcommon%=\n\t"
    "LWpre%=:\n\t"
    "s_mov_b32 s42, 73728\n\t"
    "s_mov_b32 s43, 0x20000\n\t"
    "s_sub_u32 s61, s34, 144\n\t"
    "s_mov_b32 s54, 2\n\t"
    "s_mov_b32 s55, 768\n\t"
    "v_lshrrev_b32 v41, 1, v40\n\t"
    "v_mul_u32_u24 v41, 384, v41\n\t"
    "v_and_b32 v44, 1, v40\n\t"
    "v_lshl_add_u32 v41, v44, 4, v41\n\t"
    "s_mul_i32 s56, s61, 43\n\t"
    "s_lshr_b32 s56, s56, 8\n\t"
    "s_lshl_b32 s56, s56, 5\n\t"
    "s_waitcnt lgkmcnt(0)\n\t"
    "s_mov_b32 m0, 122880\n\t"
    "s_add_u32 s26, s56, 0\n\t"
    "buffer_load_dwordx4 v41, s[40:43], s26 offen lds\n\t"
    "s_mov_b32 m0, 123904\n\t"
    "s_add_u32 s26, s56, 12288\n\t"
    "buffer_load_dwordx4 v41, s[40:43], s26 offen lds\n\t"
    "s_mov_b32 m0, 124928\n\t"
    "s_add_u32 s26, s56, 24576\n\t"
    "buffer_load_dwordx4 v41, s[40:43], s26 offen lds\n\t"
    "s_mov_b32 m0, 125952\n\t"
    "s_add_u32 s26, s56, 36864\n\t"
    "buffer_load_dwordx4 v41, s[40:43], s26 offen lds\n\t"
    "s_mov_b32 m0, 126976\n\t"
    "s_add_u32 s26, s56, 49152\n\t"
    "buffer_load_dwordx4 v41, s[40:43], s26 offen lds\n\t"
    "s_mov_b32 m0, 128000\n\t"
    "s_add_u32 s26, s56, 61440\n\t"
    "buffer_load_dwordx4 v41, s[40:43], s26 offen lds\n\t"
    "s_branch Ldec%=\n\t"
    "Lnba%=:\n\t"
    "s_mov_b32 s10, 19267584\n\t"
    "s_mov_b32 s11, 0x20000\n\t"
    "s_mov_b32 s14, 19267584\n\t"
    "s_mov_b32 s15, 0x20000\n\t"
    "s_mov_b32 s18, 210944\n\t"
    "s_mov_b32 s19, 0x20000\n\t"
    "v_and_b32 v50, 15, v40\n\t"
    "v_lshrrev_b32 v51, 4, v40\n\t"
    "v_lshrrev_b32 v55, 3, v50\n\t"
    "v_mul_u32_u24 v55, 3072, v55\n\t"
    "v_and_b32 v56, 7, v50\n\t"
    "v_lshl_add_u32 v55, v56, 7, v55\n\t"
    "v_and_b32 v56, 6, v50\n\t"
    "v_xor_b32 v56, v56, v51\n\t"
    "v_lshl_add_u32 v55, v56, 4, v55\n\t"
    "v_xor_b32 v56, 64, v55\n\t"
    "v_mov_b32 v54, 2147483648\n\t"
    "v_lshlrev_b32 v7, 4, v40\n\t"
    "s_cmp_eq_u32 s54, 2\n\t"
    "s_cbranch_scc1 LsvcW1%=\n\t"
    "v_add_u32 v41, 86016, v55\n\t"
    "v_add_u32 v44, 86016, v56\n\t"
    "s_mov_b32 s25, 0x3dd53b95\n\t"
    "v_mov_b32 v62, v7\n\t"
    "s_lshl_b32 s24, s61, 10\n\t"
    "s_lshr_b32 s26, s61, 1\n\t"
    "s_lshl_b32 s55, s26, 7\n\t"
    "s_add_u32 s55, s55, 147456\n\t"
    "s_and_b32 s26, s61, 1\n\t"
    "s_lshl_b32 s26, s26, 3\n\t"
    "s_add_u32 s55, s55, s26\n\t"
    "s_waitcnt vmcnt(12)\n\t"
    "ds_read_b128 v[112:115], v41 offset:36864\n\t"
    "ds_read_b128 v[64:67], v41 offset:0\n\t"
    "ds_read_b128 v[88:91], v41 offset:18432\n\t"
    "ds_read_b128 v[116:119], v44 offset:36864\n\t"
    "ds_read_b128 v[68:71], v44 offset:0\n\t"
    "ds_read_b128 v[92:95], v44 offset:18432\n\t"
    "s_waitcnt vmcnt(6)\n\t"
    "ds_read_b128 v[120:123], v41 offset:37888\n\t"
    "ds_read_b128 v[72:75], v41 offset:1024\n\t"
    "ds_read_b128 v[96:99], v41 offset:19456\n\t"
    "ds_read_b128 v[124:127], v44 offset:37888\n\t"
    "ds_read_b128 v[76:79], v44 offset:1024\n\t"
    "ds_read_b128 v[100:103], v44 offset:19456\n\t"
    "s_waitcnt lgkmcnt(6)\n\t"
    "v_mfma_f32_16x16x32_bf16 v[136:139], v[64:67], v[112:115], 0\n\t"
    "v_mfma_f32_16x16x32_bf16 v[140:143], v[88:91], v[112:115], 0\n\t"
    "v_mfma_f32_16x16x32_bf16 v[136:139], v[68:71], v[116:119], v[136:139]\n\t"
    "v_mfma_f32_16x16x32_bf16 v[140:143], v[92:95], v[116:119], v[140:143]\n\t"
    "s_waitcnt vmcnt(0)\n\t"
    "ds_read_b128 v[128:131], v41 offset:38912\n\t"
    "ds_read_b128 v[80:83], v41 offset:2048\n\t"
    "ds_read_b128 v[104:107], v41 offset:20480\n\t"
    "ds_read_b128 v[132:135], v44 offset:38912\n\t"
    "ds_read_b128 v[84:87], v44 offset:2048\n\t"
    "ds_read_b128 v[108:111], v44 offset:20480\n\t"
    "s_waitcnt lgkmcnt(6)\n\t"
    "v_mfma_f32_16x16x32_bf16 v[136:139], v[72:75], v[120:123], v[136:139]\n\t"
    "v_mfma_f32_16x16x32_bf16 v[140:143], v[96:99], v[120:123], v[140:143]\n\t"
    "v_mfma_f32_16x16x32_bf16 v[136:139], v[76:79], v[124:127], v[136:139]\n\t"
    "v_mfma_f32_16x16x32_bf16 v[140:143], v[100:103], v[124:127], v[140:143]\n\t"
    "s_waitcnt lgkmcnt(3)\n\t"
    "v_mfma_f32_16x16x32_bf16 v[136:139], v[80:83], v[128:131], v[136:139]\n\t"
    "v_mfma_f32_16x16x32_bf16 v[140:143], v[104:107], v[128:131], v[140:143]\n\t"
    "s_waitcnt lgkmcnt(0)\n\t"
    "v_mfma_f32_16x16x32_bf16 v[136:139], v[84:87], v[132:135], v[136:139]\n\t"
    "v_mfma_f32_16x16x32_bf16 v[140:143], v[108:111], v[132:135], v[140:143]\n\t"
    "Lfold%=:\n\t"
    "s_nop 7\n\t"
    "v_mul_f32 v136, s25, v136\n\t"
    "v_mul_f32 v137, s25, v137\n\t"
    "v_mul_f32 v138, s25, v138\n\t"
    "v_mul_f32 v139, s25, v139\n\t"
    "v_mul_f32 v140, s25, v140\n\t"
    "v_mul_f32 v141, s25, v141\n\t"
    "v_mul_f32 v142, s25, v142\n\t"
    "v_mul_f32 v143, s25, v143\n\t"
    "v_cvt_pk_bf16_f32 v144, v136, v137\n\t"
    "v_cvt_pk_bf16_f32 v145, v138, v139\n\t"
    "v_cvt_pk_bf16_f32 v146, v140, v141\n\t"
    "v_cvt_pk_bf16_f32 v147, v142, v143\n\t"
    "buffer_store_dwordx4 v[144:147], v62, s[16:19], s24 offen sc1\n\t"
    "s_cmp_eq_u32 s3, 0\n\t"
    "s_cbranch_scc1 Lnbb%=\n\t"
    "s_barrier\n\t"
    "Lnbb%=:\n\t"
    "v_mov_b32 v63, v54\n\t"
    "v_writelane_b32 v63, s55, 0\n\t"
    "s_cmp_lg_u32 s2, 0\n\t"
    "s_cbranch_scc1 Ltagok%=\n\t"
    "s_load_dwordx2 s[44:45], s[16:17], 158720\n\t"
    "s_waitcnt lgkmcnt(0)\n\t"
    "s_mul_i32 s44, s44, 0x41c64e6d\n\t"
    "s_add_u32 s44, s44, 0x3039\n\t"
    "s_mul_i32 s45, s45, 0x915f77f5\n\t"
    "s_add_u32 s45, s45, 0x9e3779b9\n\t"
    "s_xor_b32 s45, s45, s44\n\t"
    "Ltagok%=:\n\t"
    "v_mov_b32 v48, s44\n\t"
    "v_mov_b32 v49, s45\n\t"
    "s_cmp_eq_u32 s54, 2\n\t"
    "s_cbranch_scc1 Lc1%=\n\t"
    "s_waitcnt vmcnt(0)\n\t"
    "buffer_store_dwordx2 v[48:49], v63, s[16:19], 0 offen sc1\n\t"
    "v_mov_b32 v63, v54\n\t"
    "s_branch Lc1%=\n\t"
    "LsvcW1%=:\n\t"
    "v_lshrrev_b32 v41, 2, v50\n\t"
    "v_and_b32 v44, 3, v50\n\t"
    "v_lshl_add_u32 v63, v51, 3, v41\n\t"
    "v_lshlrev_b32 v63, 5, v63\n\t"
    "v_lshl_add_u32 v63, v44, 3, v63\n\t"
    "v_add_u32 v63, 122880, v63\n\t"
    "s_mov_b32 s25, 1.0\n\t"
    "s_lshr_b32 s26, s62, 2\n\t"
    "s_mul_i32 s56, s26, 6\n\t"
    "s_add_u32 s56, s56, s63\n\t"
    "s_add_u32 s56, s56, 72\n\t"
    "s_lshl_b32 s56, s56, 10\n\t"
    "s_and_b32 s27, s62, 3\n\t"
    "s_lshl_b32 s28, s27, 6\n\t"
    "s_lshr_b32 s29, s27, 1\n\t"
    "s_mul_i32 s29, s29, 36864\n\t"
    "s_and_b32 s30, s27, 1\n\t"
    "s_lshl_b32 s30, s30, 7\n\t"
    "s_add_u32 s29, s29, s30\n\t"
    "s_cmp_ge_u32 s26, 1\n\t"
    "s_cselect_b32 s28, s29, s28\n\t"
    "s_cselect_b32 s29, 1, 2\n\t"
    "s_cselect_b32 s30, 1, 3\n\t"
    "s_add_u32 s24, s56, s28\n\t"
    "v_lshrrev_b32 v41, s29, v50\n\t"
    "v_and_b32 v44, s30, v50\n\t"
    "v_mul_u32_u24 v62, 18432, v44\n\t"
    "v_lshl_add_u32 v41, v51, 4, v41\n\t"
    "v_lshl_add_u32 v62, v41, 4, v62\n\t"
    "v_add_u32 v41, 86016, v55\n\t"
    "v_add_u32 v44, 86016, v56\n\t"
    "s_lshl_b32 s55, s61, 3\n\t"
    "s_add_u32 s55, s55, 152064\n\t"
    "s_waitcnt vmcnt(0)\n\t"
    "ds_read_b64_tr_b16 v[112:113], v63 offset:0\n\t"
    "ds_read_b64_tr_b16 v[114:115], v63 offset:128\n\t"
    "ds_read_b64_tr_b16 v[116:117], v63 offset:1024\n\t"
    "ds_read_b64_tr_b16 v[118:119], v63 offset:1152\n\t"
    "ds_read_b64_tr_b16 v[120:121], v63 offset:2048\n\t"
    "ds_read_b64_tr_b16 v[122:123], v63 offset:2176\n\t"
    "ds_read_b64_tr_b16 v[124:125], v63 offset:3072\n\t"
    "ds_read_b64_tr_b16 v[126:127], v63 offset:3200\n\t"
    "ds_read_b64_tr_b16 v[128:129], v63 offset:4096\n\t"
    "ds_read_b64_tr_b16 v[130:131], v63 offset:4224\n\t"
    "ds_read_b64_tr_b16 v[132:133], v63 offset:5120\n\t"
    "ds_read_b64_tr_b16 v[134:135], v63 offset:5248\n\t"
    "ds_read_b128 v[64:67], v41 offset:0\n\t"
    "ds_read_b128 v[88:91], v41 offset:18432\n\t"
    "ds_read_b128 v[68:71], v44 offset:0\n\t"
    "ds_read_b128 v[92:95], v44 offset:18432\n\t"
    "ds_read_b128 v[72:75], v41 offset:1024\n\t"
    "ds_read_b128 v[96:99], v41 offset:19456\n\t"
    "ds_read_b128 v[76:79], v44 offset:1024\n\t"
    "ds_read_b128 v[100:103], v44 offset:19456\n\t"
    "ds_read_b128 v[80:83], v41 offset:2048\n\t"
    "ds_read_b128 v[104:107], v41 offset:20480\n\t"
    "ds_read_b128 v[84:87], v44 offset:2048\n\t"
    "ds_read_b128 v[108:111], v44 offset:20480\n\t"
    "s_waitcnt lgkmcnt(10)\n\t"
    "v_mfma_f32_16x16x32_bf16 v[136:139], v[64:67], v[112:115], 0\n\t"
    "v_mfma_f32_16x16x32_bf16 v[140:143], v[88:91], v[112:115], 0\n\t"
    "s_waitcnt lgkmcnt(8)\n\t"
    "v_mfma_f32_16x16x32_bf16 v[136:139], v[68:71], v[116:119], v[136:139]\n\t"
    "v_mfma_f32_16x16x32_bf16 v[140:143], v[92:95], v[116:119], v[140:143]\n\t"
    "s_waitcnt lgkmcnt(6)\n\t"
    "v_mfma_f32_16x16x32_bf16 v[136:139], v[72:75], v[120:123], v[136:139]\n\t"
    "v_mfma_f32_16x16x32_bf16 v[140:143], v[96:99], v[120:123], v[140:143]\n\t"
    "s_waitcnt lgkmcnt(4)\n\t"
    "v_mfma_f32_16x16x32_bf16 v[136:139], v[76:79], v[124:127], v[136:139]\n\t"
    "v_mfma_f32_16x16x32_bf16 v[140:143], v[100:103], v[124:127], v[140:143]\n\t"
    "s_waitcnt lgkmcnt(2)\n\t"
    "v_mfma_f32_16x16x32_bf16 v[136:139], v[80:83], v[128:131], v[136:139]\n\t"
    "v_mfma_f32_16x16x32_bf16 v[140:143], v[104:107], v[128:131], v[140:143]\n\t"
    "s_waitcnt lgkmcnt(0)\n\t"
    "v_mfma_f32_16x16x32_bf16 v[136:139], v[84:87], v[132:135], v[136:139]\n\t"
    "v_mfma_f32_16x16x32_bf16 v[140:143], v[108:111], v[132:135], v[140:143]\n\t"
    "s_branch Lfold%=\n\t"
    "Lnia%=:\n\t"
    "v_and_b32 v50, 15, v40\n\t"
    "v_lshrrev_b32 v51, 4, v40\n\t"
    "v_lshrrev_b32 v55, 3, v50\n\t"
    "v_mul_u32_u24 v55, 3072, v55\n\t"
    "v_and_b32 v56, 7, v50\n\t"
    "v_lshl_add_u32 v55, v56, 7, v55\n\t"
    "v_and_b32 v56, 6, v50\n\t"
    "v_xor_b32 v56, v56, v51\n\t"
    "v_lshl_add_u32 v55, v56, 4, v55\n\t"
    "v_xor_b32 v56, 64, v55\n\t"
    "v_mov_b32 v54, 2147483648\n\t"
    "v_lshlrev_b32 v7, 4, v40\n\t"
    "s_cmp_eq_u32 s3, 0\n\t"
    "s_cbranch_scc1 Lnib%=\n\t"
    "s_barrier\n\t"
    "Lnib%=:\n\t"
    "s_waitcnt lgkmcnt(0)\n\t"
    "s_mul_i32 s44, s44, 0x41c64e6d\n\t"
    "s_add_u32 s44, s44, 0x3039\n\t"
    "s_mul_i32 s45, s45, 0x915f77f5\n\t"
    "s_add_u32 s45, s45, 0x9e3779b9\n\t"
    "s_xor_b32 s45, s45, s44\n\t"
    "v_mov_b32 v63, v54\n\t"
    "Lc1%=:\n\t"
    "s_mul_i32 s48, s53, 602112\n\t"
    "s_mul_i32 s26, s59, 10752\n\t"
    "s_add_u32 s48, s48, s26\n\t"
    "s_mov_b32 s52, 83328\n\t"
    "v_mul_u32_u24 v37, 37, v42\n\t"
    "v_mul_u32_u24 v38, 384, v42\n\t"
    "v_add_u32 v38, v38, v43\n\t"
    "v_add_u32 v39, 0, v37\n\t"
    "v_lshrrev_b32 v39, 8, v39\n\t"
    "v_mad_u32_u24 v0, v39, s52, v38\n\t"
    "v_add_u32 v39, 296, v37\n\t"
    "v_lshrrev_b32 v39, 8, v39\n\t"
    "v_mad_u32_u24 v1, v39, s52, v38\n\t"
    "v_add_u32 v39, 592, v37\n\t"
    "v_lshrrev_b32 v39, 8, v39\n\t"
    "v_mad_u32_u24 v2, v39, s52, v38\n\t"
    "v_add_u32 v39, 888, v37\n\t"
    "v_lshrrev_b32 v39, 8, v39\n\t"
    "v_mad_u32_u24 v3, v39, s52, v38\n\t"
    "Lcommon%=:\n\t"
    "s_mov_b32 s1, s22\n\t"
    "v_add_u32 v39, 1184, v37\n\t"
    "v_lshrrev_b32 v39, 8, v39\n\t"
    "v_mad_u32_u24 v4, v39, s52, v38\n\t"
    "v_add_u32 v39, 1480, v37\n\t"
    "v_lshrrev_b32 v39, 8, v39\n\t"
    "v_mad_u32_u24 v5, v39, s52, v38\n\t"
    "s_mul_i32 s49, s35, 2688\n\t"
    "s_add_u32 s49, s49, s48\n\t"
    "s_mul_i32 s20, s35, 18432\n\t"
    "s_add_u32 s20, s20, 73728\n\t"
    "v_add_u32 v52, s35, v50\n\t"
    "v_and_b32 v52, 3, v52\n\t"
    "v_mul_u32_u24 v53, 2688, v52\n\t"
    "v_lshl_add_u32 v53, v51, 4, v53\n\t"
    "v_add_u32 v53, 518400, v53\n\t"
    "v_cmp_gt_u32 s[46:47], 4, v50\n\t"
    "s_mul_i32 s21, s35, 3072\n\t"
    "v_add_u32 v8, 65536, v7\n\t"
    "v_cndmask_b32 v6, v54, v53, s[46:47]\n\t"
    "v_add_u32 v9, 131072, v7\n\t"
    "v_add_u32 v13, s20, v7\n\t"
    "v_subrev_u32 v12, 73728, v13\n\t"
    "v_add_u32 v14, s21, v7\n\t"
    "v_add_u32 v10, s20, v55\n\t"
    "v_add_u32 v11, s20, v56\n\t"
    "s_add_u32 s50, s49, 6144\n\t"
    "s_add_u32 s51, s49, 12288\n\t"
    "v_mul_u32_u24 v57, 148, v51\n\t"
    "v_mul_u32_u24 v58, 1536, v51\n\t"
    "v_lshl_add_u32 v58, v50, 3, v58\n\t"
    "v_add_u32 v47, 0, v57\n\t"
    "v_lshrrev_b32 v47, 8, v47\n\t"
    "v_mad_u32_u24 v16, v47, s52, v58\n\t"
    "v_add_u32 v47, 37, v57\n\t"
    "v_lshrrev_b32 v47, 8, v47\n\t"
    "v_mad_u32_u24 v17, v47, s52, v58\n\t"
    "v_add_u32 v47, 74, v57\n\t"
    "v_lshrrev_b32 v47, 8, v47\n\t"
    "v_mad_u32_u24 v18, v47, s52, v58\n\t"
    "v_add_u32 v47, 111, v57\n\t"
    "v_lshrrev_b32 v47, 8, v47\n\t"
    "v_mad_u32_u24 v19, v47, s52, v58\n\t"
    "v_add_u32 v47, 592, v57\n\t"
    "v_lshrrev_b32 v47, 8, v47\n\t"
    "v_mad_u32_u24 v20, v47, s52, v58\n\t"
    "v_add_u32 v47, 629, v57\n\t"
    "v_lshrrev_b32 v47, 8, v47\n\t"
    "v_mad_u32_u24 v21, v47, s52, v58\n\t"
    "v_add_u32 v47, 666, v57\n\t"
    "v_lshrrev_b32 v47, 8, v47\n\t"
    "v_mad_u32_u24 v22, v47, s52, v58\n\t"
    "v_add_u32 v47, 703, v57\n\t"
    "v_lshrrev_b32 v47, 8, v47\n\t"
    "v_mad_u32_u24 v23, v47, s52, v58\n\t"
    "v_add_u32 v47, 1184, v57\n\t"
    "v_lshrrev_b32 v47, 8, v47\n\t"
    "v_mad_u32_u24 v24, v47, s52, v58\n\t"
    "v_add_u32 v47, 1221, v57\n\t"
    "v_lshrrev_b32 v47, 8, v47\n\t"
    "v_mad_u32_u24 v25, v47, s52, v58\n\t"
    "v_add_u32 v47, 1258, v57\n\t"
    "v_lshrrev_b32 v47, 8, v47\n\t"
    "v_mad_u32_u24 v26, v47, s52, v58\n\t"
    "v_add_u32 v47, 1295, v57\n\t"
    "v_lshrrev_b32 v47, 8, v47\n\t"
    "v_mad_u32_u24 v27, v47, s52, v58\n\t"
    "s_mov_b64 s[22:23], 0xffff\n\t"
    "v_lshlrev_b32 v47, 3, v50\n\t"
    "v_add_u32 v47, 518400, v47\n\t"
    "v_cndmask_b32 v28, v54, v47, s[22:23]\n\t"
    "v_lshlrev_b32 v59, 4, v51\n\t"
    "v_lshl_add_u32 v29, v52, 6, v59\n\t"
    "v_add_u32 v29, 147456, v29\n\t"
    "v_add_u32 v60, 152064, v7\n\t"
    "s_lshl_b32 s26, s35, 6\n\t"
    "s_add_u32 s26, s26, 148992\n\t"
    "v_mul_u32_u24 v47, 768, v52\n\t"
    "v_add3_u32 v47, v47, v59, s26\n\t"
    "v_cndmask_b32 v30, v60, v47, s[46:47]\n\t"
    "s_mul_i32 s26, s35, 768\n\t"
    "s_add_u32 s26, s26, 148992\n\t"
    "v_lshl_add_u32 v31, v50, 2, s26\n\t"
    "s_and_b32 s27, s35, 1\n\t"
    "s_lshr_b32 s26, s35, 1\n\t"
    "s_lshl_b32 s26, s26, 8\n\t"
    "s_lshl_b32 s27, s27, 3\n\t"
    "s_add_u32 s26, s26, s27\n\t"
    "v_add_u32 v47, s26, v29\n\t"
    "v_cndmask_b32 v32, v60, v47, s[46:47]\n\t"
    "s_lshl_b32 s26, s21, 1\n\t"
    "v_add_u32 v33, s26, v7\n\t"
    "v_mov_b32 v35, 0xff800000\n\t"
    "v_lshlrev_b32 v47, 2, v50\n\t"
    "v_sub_u32 v243, v16, v47\n\t"
    "v_sub_u32 v244, v17, v47\n\t"
    "v_sub_u32 v245, v18, v47\n\t"
    "v_sub_u32 v246, v19, v47\n\t"
    "v_sub_u32 v247, v20, v47\n\t"
    "v_sub_u32 v248, v21, v47\n\t"
    "v_sub_u32 v249, v22, v47\n\t"
    "v_sub_u32 v250, v23, v47\n\t"
    "v_sub_u32 v251, v24, v47\n\t"
    "v_sub_u32 v252, v25, v47\n\t"
    "v_sub_u32 v253, v26, v47\n\t"
    "v_sub_u32 v254, v27, v47\n\t"
    "v_sub_u32 v255, v28, v47\n\t"
    "v_mov_b32 v39, 154176\n\t"
    "v_and_b32 v47, 3, v50\n\t"
    "v_cmp_eq_u32 vcc, 0, v47\n\t"
    "s_lshl_b32 s26, s35, 7\n\t"
    "s_add_u32 s26, s26, 155712\n\t"
    "v_cndmask_b32 v29, v39, v29, vcc\n\t"
    "v_cmp_eq_u32 vcc, 0, v50\n\t"
    "v_add_u32 v38, s26, v59\n\t"
    "v_lshlrev_b32 v37, 1, v47\n\t"
    "v_cndmask_b32 v38, v39, v38, vcc\n\t"
    "v_and_b32 v47, 1, v51\n\t"
    "v_lshl_add_u32 v37, v47, 3, v37\n\t"
    "v_lshrrev_b32 v47, 2, v50\n\t"
    "v_lshl_add_u32 v37, v47, 4, v37\n\t"
    "v_lshrrev_b32 v47, 1, v51\n\t"
    "v_lshl_add_u32 v37, v47, 6, v37\n\t"
    "v_add_u32 v37, s26, v37\n\t"
    "s_mov_b32 s27, 73728\n\t"
    "s_mov_b32 s28, 77824\n\t"
    "Lst0%=:\n\t"
    "s_cbranch_execz Lst1%=\n\t"
    "s_mov_b32 s29, 81920\n\t"
    "s_mov_b32 s30, 86016\n\t"
    "s_mov_b32 s31, 90112\n\t"
    "s_mov_b32 s60, 156672\n\t"
    "s_mov_b32 s61, 152064\n\t"
    "s_mov_b32 s58, 200000\n\t"
    "s_add_u32 s53, s49, 15360\n\t"
    "buffer_load_dwordx4 a[72:75], v6, s[8:11], s48 offen offset:0\n\t"
    "buffer_load_dwordx4 a[76:79], v6, s[8:11], s48 offen offset:64\n\t"
    "buffer_load_dwordx4 a[80:83], v6, s[8:11], s48 offen offset:128\n\t"
    "buffer_load_dwordx4 a[84:87], v6, s[8:11], s48 offen offset:192\n\t"
    "buffer_load_dwordx4 a[88:91], v6, s[8:11], s48 offen offset:256\n\t"
    "buffer_load_dwordx4 a[92:95], v6, s[8:11], s48 offen offset:320\n\t"
    "v_cmp_gt_u32 s[46:47], 36, v40\n\t"
    "s_mov_b32 s57, 15\n\t"
    "s_mov_b32 s56, -1\n\t"
    "v_cndmask_b32 v0, v54, v7, s[46:47]\n\t"
    "s_cmp_eq_u32 s35, 0\n\t"
    "s_cbranch_scc1 Lsvc2%=\n\t"
    "v_mov_b32 v64, 0\n\t"
    "v_mov_b32 v65, 0\n\t"
    "v_lshrrev_b32 v47, 1, v7\n\t"
    "s_lshl_b32 s26, s35, 9\n\t"
    "s_add_u32 s26, s26, 153664\n\t"
    "v_add_u32 v47, s26, v47\n\t"
    "ds_write_b64 v47, v[64:65]\n\t"
    "v_mov_b32 v1, v54\n\t"
    "v_mov_b32 v61, v54\n\t"
    "s_cmp_eq_u32 s35, 3\n\t"
    "s_cbranch_scc1 Lw3%=\n\t"
    "s_waitcnt vmcnt(6)\n\t"
    "s_branch Ljoin2%=\n\t"
    "Lw3%=:\n\t"
    "s_sub_u32 s26, s34, 144\n\t"
    "s_cmp_ge_u32 s26, 72\n\t"
    "s_cbranch_scc1 Lw3b%=\n\t"
    "s_waitcnt vmcnt(6)\n\t"
    "buffer_store_dwordx2 v[48:49], v63, s[16:19], 0 offen sc1\n\t"
    "Lw3b%=:\n\t"
    "s_branch Ljoin2%=\n\t"
    "Lsvc2%=:\n\t"
    "s_mov_b64 exec, 0\n\t"
    "s_branch Lst0%=\n\t"
    "Lstret%=:\n\t"
    "s_mov_b64 exec, -1\n\t"
    "s_lshl_b32 s26, s34, 3\n\t"
    "s_add_u32 s26, s26, 156672\n\t"
    "v_mov_b32 v61, v54\n\t"
    "v_writelane_b32 v61, s26, 0\n\t"
    "v_mov_b32 v48, s44\n\t"
    "v_mov_b32 v49, s45\n\t"
    "s_mov_b32 s26, 147456\n\t"
    "s_mov_b64 s[62:63], 0\n\t"
    "v_lshrrev_b32 v47, 0, v40\n\t"
    "v_lshlrev_b32 v47, 7, v47\n\t"
    "v_and_b32 v1, 0, v40\n\t"
    "v_lshl_add_u32 v47, v1, 4, v47\n\t"
    "v_cndmask_b32 v47, v54, v47, s[56:57]\n\t"
    "s_cmp_eq_u32 s34, 0\n\t"
    "s_cselect_b64 s[46:47], -1, 0\n\t"
    "v_cndmask_b32 v1, v54, v7, s[46:47]\n\t"
    "s_getreg_b32 s59, hwreg(HW_REG_XCC_ID)\n\t"
    "s_and_b32 s59, s59, 4\n\t"
    "s_mov_b32 s59, 160\n\t"
    "s_cselect_b32 s59, s59, 200\n\t"
    "Lpace%=:\n\t"
    "s_memrealtime s[54:55]\n\t"
    "s_waitcnt lgkmcnt(0)\n\t"
    "s_sub_u32 s54, s54, s1\n\t"
    "s_cmp_lt_u32 s54, s59\n\t"
    "s_cbranch_scc1 Lpace%=\n\t"
    "Lpoll%=:\n\t"
    "buffer_load_dwordx4 v[64:67], v47, s[16:19], s26 offen sc1\n\t"
    "s_waitcnt vmcnt(0)\n\t"
    "v_cmp_eq_u64 vcc, s[44:45], v[64:65]\n\t"
    "v_cmp_eq_u64 s[46:47], s[44:45], v[66:67]\n\t"
    "s_sub_u32 s58, s58, 1\n\t"
    "s_nop 0\n\t"
    "s_and_b64 vcc, vcc, s[46:47]\n\t"
    "s_or_b64 s[62:63], s[62:63], vcc\n\t"
    "s_and_b64 s[62:63], s[62:63], s[56:57]\n\t"
    "s_cmp_eq_u64 s[62:63], s[56:57]\n\t"
    "s_cbranch_scc1 Lgot%=\n\t"
    "v_cndmask_b32 v47, v47, v54, s[62:63]\n\t"
    "s_cmp_lg_u32 s58, 199988\n\t"
    "s_cbranch_scc1 Lnofb%=\n\t"
    "s_or_b32 s54, s3, s2\n\t"
    "s_cbranch_scc1 Lnofb%=\n\t"
    "s_cmp_ge_u32 s34, 144\n\t"
    "s_cbranch_scc1 Lnofb%=\n\t"
    "s_mov_b32 s2, 1\n\t"
    "s_branch Lrestart%=\n\t"
    "Lnofb%=:\n\t"
    "s_sleep 4\n\t"
    "s_cmp_lg_u32 s58, 0\n\t"
    "s_cbranch_scc1 Lpoll%=\n\t"
    "Lgot%=:\n\t"
    "s_mov_b32 m0, 0\n\t"
    "s_mov_b32 s26, 0\n\t"
    "buffer_load_dwordx4 v7, s[16:19], s26 offen offset:0 sc1 lds\n\t"
    "buffer_load_dwordx4 v7, s[16:19], s26 offen offset:1024 sc1 lds\n\t"
    "buffer_load_dwordx4 v7, s[16:19], s26 offen offset:2048 sc1 lds\n\t"
    "buffer_load_dwordx4 v7, s[16:19], s26 offen offset:3072 sc1 lds\n\t"
    "s_mov_b32 m0, 4096\n\t"
    "s_mov_b32 s26, 4096\n\t"
    "buffer_load_dwordx4 v7, s[16:19], s26 offen offset:0 sc1 lds\n\t"
    "buffer_load_dwordx4 v7, s[16:19], s26 offen offset:1024 sc1 lds\n\t"
    "buffer_load_dwordx4 v7, s[16:19], s26 offen offset:2048 sc1 lds\n\t"
    "buffer_load_dwordx4 v7, s[16:19], s26 offen offset:3072 sc1 lds\n\t"
    "s_mov_b32 m0, 8192\n\t"
    "s_mov_b32 s26, 8192\n\t"
    "buffer_load_dwordx4 v7, s[16:19], s26 offen offset:0 sc1 lds\n\t"
    "buffer_load_dwordx4 v7, s[16:19], s26 offen offset:1024 sc1 lds\n\t"
    "buffer_load_dwordx4 v7, s[16:19], s26 offen offset:2048 sc1 lds\n\t"
    "buffer_load_dwordx4 v7, s[16:19], s26 offen offset:3072 sc1 lds\n\t"
    "Ljoin2%=:\n\t"
    "s_mov_b32 s36, 0xffff0000\n\t"
    "s_mov_b32 s37, 0\n\t"
    "s_mov_b32 s38, 0\n\t"
    "s_mov_b32 s39, 0xffff\n\t"
    "s_mov_b32 s40, 0\n\t"
    "s_mov_b32 s41, 0x10000\n\t"
    "s_or_b32 s42, s34, s35\n\t"
    "s_barrier\n\t"
    "ds_read_b128 a[0:3], v10 offset:0\n\t"
    "ds_read_b128 a[4:7], v11 offset:0\n\t"
    "ds_read_b128 a[8:11], v10 offset:1024\n\t"
    "ds_read_b128 a[12:15], v11 offset:1024\n\t"
    "ds_read_b128 a[16:19], v10 offset:2048\n\t"
    "ds_read_b128 a[20:23], v11 offset:2048\n\t"
    "ds_read_b128 a[24:27], v10 offset:6144\n\t"
    "ds_read_b128 a[28:31], v11 offset:6144\n\t"
    "ds_read_b128 a[32:35], v10 offset:7168\n\t"
    "ds_read_b128 a[36:39], v11 offset:7168\n\t"
    "ds_read_b128 a[40:43], v10 offset:8192\n\t"
    "ds_read_b128 a[44:47], v11 offset:8192\n\t"
    "; (all earlier vmem)\n\t"
    "s_add_u32 m0, s21, 12288\n\t"
    "s_mov_b32 s26, 12288\n\t"
    "buffer_load_dwordx4 v14, s[16:19], s26 offen offset:0 sc1 lds\n\t"
    "buffer_load_dwordx4 v14, s[16:19], s26 offen offset:1024 sc1 lds\n\t"
    "buffer_load_dwordx4 v14, s[16:19], s26 offen offset:2048 sc1 lds\n\t"
    "s_add_u32 m0, s21, 24576\n\t"
    "s_mov_b32 s26, 24576\n\t"
    "buffer_load_dwordx4 v14, s[16:19], s26 offen offset:0 sc1 lds\n\t"
    "buffer_load_dwordx4 v14, s[16:19], s26 offen offset:1024 sc1 lds\n\t"
    "buffer_load_dwordx4 v14, s[16:19], s26 offen offset:2048 sc1 lds\n\t"
    "buffer_store_dwordx2 v[48:49], v61, s[16:19], 0 offen sc1\n\t"
    "s_cmp_lg_u32 s35, 0\n\t"
    "s_cbranch_scc1 Lnw%=\n\t"
    "s_waitcnt vmcnt(7)\n\t"
    "Lnw%=:\n\t"
    "s_waitcnt lgkmcnt(0)\n\t"
    "s_barrier\n\t"
    "ds_read_b128 a[96:99], v7 offset:0\n\t"
    "ds_read_b128 a[100:103], v7 offset:1024\n\t"
    "ds_read_b128 a[104:107], v7 offset:2048\n\t"
    "ds_read_b128 a[108:111], v7 offset:3072\n\t"
    "ds_read_b128 a[112:115], v7 offset:4096\n\t"
    "ds_read_b128 a[116:119], v7 offset:5120\n\t"
    "ds_read_b128 a[120:123], v7 offset:6144\n\t"
    "ds_read_b128 a[124:127], v7 offset:7168\n\t"
    "s_waitcnt lgkmcnt(7)\n\t"
    "v_mfma_f32_16x16x32_bf16 v[40:43], a[96:99], a[0:3], 0\n\t"
    "s_add_u32 m0, s21, 36864\n\t"
    "s_mov_b32 s26, 36864\n\t"
    "v_mfma_f32_16x16x32_bf16 v[44:47], a[96:99], a[24:27], 0\n\t"
    "ds_read_b128 a[128:131], v7 offset:8192\n\t"
    "s_waitcnt lgkmcnt(7)\n\t"
    "v_mfma_f32_16x16x32_bf16 v[40:43], a[100:103], a[4:7], v[40:43]\n\t"
    "buffer_load_dwordx4 v14, s[16:19], s26 offen offset:0 sc1 lds\n\t"
    "v_mfma_f32_16x16x32_bf16 v[44:47], a[100:103], a[28:31], v[44:47]\n\t"
    "ds_read_b128 a[96:99], v7 offset:9216\n\t"
    "s_waitcnt lgkmcnt(7)\n\t"
    "v_mfma_f32_16x16x32_bf16 v[40:43], a[104:107], a[8:11], v[40:43]\n\t"
    "ds_read_b128 a[132:135], v7 offset:10240\n\t"
    "v_mov_b32 v55, 0\n\t"
    "v_mfma_f32_16x16x32_bf16 v[44:47], a[104:107], a[32:35], v[44:47]\n\t"
    "ds_read_b128 a[100:103], v7 offset:11264\n\t"
    "s_waitcnt lgkmcnt(4)\n\t"
    "v_mfma_f32_16x16x32_bf16 v[40:43], a[108:111], a[12:15], v[40:43]\n\t"
    "buffer_load_dwordx4 v14, s[16:19], s26 offen offset:1024 sc1 lds\n\t"
    "v_mfma_f32_16x16x32_bf16 v[44:47], a[108:111], a[36:39], v[44:47]\n\t"
    "v_mov_b32 v59, 0\n\t"
    "v_mov_b32 v67, 0\n\t"
    "v_mfma_f32_16x16x32_bf16 v[40:43], a[112:115], a[16:19], v[40:43]\n\t"
    "v_mfma_f32_16x16x32_bf16 v[44:47], a[112:115], a[40:43], v[44:47]\n\t"
    "buffer_load_dwordx4 v14, s[16:19], s26 offen offset:2048 sc1 lds\n\t"
    "v_mfma_f32_16x16x32_bf16 v[40:43], a[116:119], a[20:23], v[40:43]\n\t"
    "s_add_u32 m0, s21, 49152\n\t"
    "s_mov_b32 s26, 49152\n\t"
    "v_mfma_f32_16x16x32_bf16 v[44:47], a[116:119], a[44:47], v[44:47]\n\t"
    "s_waitcnt vmcnt(7)\n\t"
    "s_barrier\n\t"
    "v_mfma_f32_16x16x32_bf16 v[68:71], a[120:123], a[0:3], 0\n\t"
    "buffer_load_dwordx4 v14, s[16:19], s26 offen offset:0 sc1 lds\n\t"
    "v_mfma_f32_16x16x32_bf16 v[72:75], a[120:123], a[24:27], 0\n\t"
    "ds_read_b128 a[104:107], v7 offset:12288\n\t"
    "v_cvt_pk_bf16_f32 v76, v40, v41\n\t"
    "v_mfma_f32_16x16x32_bf16 v[68:71], a[124:127], a[4:7], v[68:71]\n\t"
    "ds_read_b128 a[108:111], v7 offset:13312\n\t"
    "v_cvt_pk_bf16_f32 v77, v42, v43\n\t"
    "v_mfma_f32_16x16x32_bf16 v[72:75], a[124:127], a[28:31], v[72:75]\n\t"
    "ds_read_b128 a[112:115], v7 offset:14336\n\t"
    "s_waitcnt lgkmcnt(6)\n\t"
    "v_mfma_f32_16x16x32_bf16 v[68:71], a[128:131], a[8:11], v[68:71]\n\t"
    "buffer_load_dwordx4 v14, s[16:19], s26 offen offset:1024 sc1 lds\n\t"
    "v_mfma_f32_16x16x32_bf16 v[72:75], a[128:131], a[32:35], v[72:75]\n\t"
    "ds_read_b128 a[116:119], v7 offset:15360\n\t"
    "s_waitcnt lgkmcnt(5)\n\t"
    "v_mfma_f32_16x16x32_bf16 v[68:71], a[96:99], a[12:15], v[68:71]\n\t"
    "ds_read_b128 a[120:123], v7 offset:16384\n\t"
    "v_cvt_pk_bf16_f32 v40, v44, v45\n\t"
    "v_mfma_f32_16x16x32_bf16 v[72:75], a[96:99], a[36:39], v[72:75]\n\t"
    "buffer_load_dwordx4 v14, s[16:19], s26 offen offset:2048 sc1 lds\n\t"
    "v_mfma_f32_16x16x32_bf16 v[68:71], a[132:135], a[16:19], v[68:71]\n\t"
    "s_add_u32 m0, s21, 61440\n\t"
    "s_mov_b32 s26, 61440\n\t"
    "v_mfma_f32_16x16x32_bf16 v[72:75], a[132:135], a[40:43], v[72:75]\n\t"
    "ds_read_b128 a[96:99], v7 offset:17408\n\t"
    "s_waitcnt lgkmcnt(6)\n\t"
    "v_mfma_f32_16x16x32_bf16 v[68:71], a[100:103], a[20:23], v[68:71]\n\t"
    "buffer_load_dwordx4 v14, s[16:19], s26 offen offset:0 sc1 lds\n\t"
    "v_mfma_f32_16x16x32_bf16 v[72:75], a[100:103], a[44:47], v[72:75]\n\t"
    "ds_read_b128 a[124:127], v7 offset:18432\n\t"
    "s_waitcnt lgkmcnt(6)\n\t"
    "v_mfma_f32_16x16x32_bf16 v[80:83], a[104:107], a[0:3], 0\n\t"
    "ds_read_b128 a[128:131], v7 offset:19456\n\t"
    "v_cvt_pk_bf16_f32 v41, v46, v47\n\t"
    "v_mfma_f32_16x16x32_bf16 v[84:87], a[104:107], a[24:27], 0\n\t"
    "ds_read_b128 a[100:103], v7 offset:20480\n\t"
    "s_waitcnt lgkmcnt(7)\n\t"
    "v_mfma_f32_16x16x32_bf16 v[80:83], a[108:111], a[4:7], v[80:83]\n\t"
    "buffer_load_dwordx4 v14, s[16:19], s26 offen offset:1024 sc1 lds\n\t"
    "v_mfma_f32_16x16x32_bf16 v[84:87], a[108:111], a[28:31], v[84:87]\n\t"
    "ds_read_b128 a[104:107], v7 offset:21504\n\t"
    "s_waitcnt lgkmcnt(7)\n\t"
    "v_mfma_f32_16x16x32_bf16 v[80:83], a[112:115], a[8:11], v[80:83]\n\t"
    "ds_read_b128 a[132:135], v7 offset:22528\n\t"
    "v_cvt_pk_bf16_f32 v78, v68, v69\n\t"
    "v_mfma_f32_16x16x32_bf16 v[84:87], a[112:115], a[32:35], v[84:87]\n\t"
    "ds_read_b128 a[108:111], v7 offset:23552\n\t"
    "s_waitcnt lgkmcnt(8)\n\t"
    "v_mfma_f32_16x16x32_bf16 v[80:83], a[116:119], a[12:15], v[80:83]\n\t"
    "buffer_load_dwordx4 v14, s[16:19], s26 offen offset:2048 sc1 lds\n\t"
    "v_mfma_f32_16x16x32_bf16 v[84:87], a[116:119], a[36:39], v[84:87]\n\t"
    "s_add_u32 m0, s20, 12288\n\t"
    "s_waitcnt lgkmcnt(7)\n\t"
    "v_mfma_f32_16x16x32_bf16 v[80:83], a[120:123], a[16:19], v[80:83]\n\t"
    "ds_read_b128 a[112:115], v33 offset:0\n\t"
    "v_cvt_pk_bf16_f32 v79, v70, v71\n\t"
    "v_mfma_f32_16x16x32_bf16 v[84:87], a[120:123], a[40:43], v[84:87]\n\t"
    "ds_read_b128 a[116:119], v33 offset:1024\n\t"
    "s_waitcnt lgkmcnt(8)\n\t"
    "v_mfma_f32_16x16x32_bf16 v[44:47], a[0:3], v[76:79], 0\n\t"
    "buffer_load_dwordx4 v4, s[8:11], s51 offen offset:0 lds\n\t"
    "v_mfma_f32_16x16x32_bf16 v[68:71], a[24:27], v[76:79], 0\n\t"
    "ds_read_b128 a[120:123], v33 offset:2048\n\t"
    "s_add_u32 m0, s20, 13184\n\t"
    "v_mfma_f32_16x16x32_bf16 v[88:91], a[72:75], v[76:79], 0\n\t"
    "ds_read_b128 a[136:139], v33 offset:3072\n\t"
    "v_cvt_pk_bf16_f32 v42, v72, v73\n\t"
    "v_mfma_f32_16x16x32_bf16 v[80:83], a[96:99], a[20:23], v[80:83]\n\t"
    "buffer_load_dwordx4 v4, s[8:11], s51 offen offset:128 lds\n\t"
    "v_mfma_f32_16x16x32_bf16 v[84:87], a[96:99], a[44:47], v[84:87]\n\t"
    "s_add_u32 m0, s20, 14080\n\t"
    "s_waitcnt lgkmcnt(7)\n\t"
    "v_mfma_f32_16x16x32_bf16 v[92:95], a[124:127], a[0:3], 0\n\t"
    "ds_read_b128 a[140:143], v33 offset:4096\n\t"
    "v_cvt_pk_bf16_f32 v43, v74, v75\n\t"
    "v_mfma_f32_16x16x32_bf16 v[96:99], a[124:127], a[24:27], 0\n\t"
    "buffer_load_dwordx4 v4, s[8:11], s51 offen offset:256 lds\n\t"
    "v_mfma_f32_16x16x32_bf16 v[100:103], a[0:3], v[40:43], 0\n\t"
    "ds_read_b128 a[96:99], v33 offset:5120\n\t"
    "s_add_u32 m0, s20, 15360\n\t"
    "v_mfma_f32_16x16x32_bf16 v[72:75], a[24:27], v[40:43], 0\n\t"
    "v_cvt_pk_bf16_f32 v104, v80, v81\n\t"
    "v_cvt_pk_bf16_f32 v105, v82, v83\n\t"
    "v_mfma_f32_16x16x32_bf16 v[108:111], a[72:75], v[40:43], 0\n\t"
    "buffer_load_dwordx4 v5, s[8:11], s53 offen offset:0 lds\n\t"
    "v_mfma_f32_16x16x32_bf16 v[92:95], a[128:131], a[4:7], v[92:95]\n\t"
    "s_add_u32 m0, s20, 16256\n\t"
    "v_cvt_pk_bf16_f32 v80, v84, v85\n\t"
    "v_mfma_f32_16x16x32_bf16 v[96:99], a[128:131], a[28:31], v[96:99]\n\t"
    "v_cvt_pk_bf16_f32 v81, v86, v87\n\t"
    "s_waitcnt lgkmcnt(5)\n\t"
    "v_mfma_f32_16x16x32_bf16 v[92:95], a[100:103], a[8:11], v[92:95]\n\t"
    "buffer_load_dwordx4 v5, s[8:11], s53 offen offset:128 lds\n\t"
    "v_mfma_f32_16x16x32_bf16 v[96:99], a[100:103], a[32:35], v[96:99]\n\t"
    "s_waitcnt vmcnt(11)\n\t"
    "s_barrier\n\t"
    "v_mfma_f32_16x16x32_bf16 v[92:95], a[104:107], a[12:15], v[92:95]\n\t"
    "ds_read_b128 a[124:127], v7 offset:24576\n\t"
    "s_add_u32 m0, s20, 17152\n\t"
    "v_mfma_f32_16x16x32_bf16 v[96:99], a[104:107], a[36:39], v[96:99]\n\t"
    "buffer_load_dwordx4 v5, s[8:11], s53 offen offset:256 lds\n\t"
    "v_mfma_f32_16x16x32_bf16 v[92:95], a[132:135], a[16:19], v[92:95]\n\t"
    "ds_read_b128 a[100:103], v7 offset:25600\n\t"
    "ds_read_b128 a[128:131], v7 offset:26624\n\t"
    "v_mfma_f32_16x16x32_bf16 v[96:99], a[132:135], a[40:43], v[96:99]\n\t"
    "ds_read_b128 a[104:107], v7 offset:27648\n\t"
    "v_mfma_f32_16x16x32_bf16 v[92:95], a[108:111], a[20:23], v[92:95]\n\t"
    "ds_read_b128 a[144:147], v7 offset:28672\n\t"
    "s_waitcnt lgkmcnt(6)\n\t"
    "v_mfma_f32_16x16x32_bf16 v[96:99], a[108:111], a[44:47], v[96:99]\n\t"
    "ds_read_b128 a[132:135], v7 offset:29696\n\t"
    "v_mfma_f32_16x16x32_bf16 v[84:87], a[112:115], a[72:75], 0\n\t"
    "ds_read_b128 a[148:151], v7 offset:30720\n\t"
    "v_mfma_f32_16x16x32_bf16 v[84:87], a[116:119], a[76:79], v[84:87]\n\t"
    "ds_read_b128 a[108:111], v7 offset:31744\n\t"
    "v_cvt_pk_bf16_f32 v106, v92, v93\n\t"
    "v_mfma_f32_16x16x32_bf16 v[84:87], a[120:123], a[80:83], v[84:87]\n\t"
    "ds_read_b128 a[112:115], v7 offset:32768\n\t"
    "v_cvt_pk_bf16_f32 v107, v94, v95\n\t"
    "v_mfma_f32_16x16x32_bf16 v[84:87], a[136:139], a[84:87], v[84:87]\n\t"
    "ds_read_b128 a[116:119], v7 offset:33792\n\t"
    "v_cvt_pk_bf16_f32 v82, v96, v97\n\t"
    "v_mfma_f32_16x16x32_bf16 v[44:47], a[4:7], v[104:107], v[44:47]\n\t"
    "ds_read_b128 a[120:123], v7 offset:34816\n\t"
    "v_cvt_pk_bf16_f32 v83, v98, v99\n\t"
    "v_mfma_f32_16x16x32_bf16 v[84:87], a[140:143], a[88:91], v[84:87]\n\t"
    "ds_read_b128 a[136:139], v7 offset:35840\n\t"
    "s_waitcnt lgkmcnt(12)\n\t"
    "v_mfma_f32_16x16x32_bf16 v[68:71], a[28:31], v[104:107], v[68:71]\n\t"
    "ds_read_b128 a[152:155], v7 offset:36864\n\t"
    "v_mfma_f32_16x16x32_bf16 v[84:87], a[96:99], a[92:95], v[84:87]\n\t"
    "ds_read_b128 a[140:143], v7 offset:37888\n\t"
    "s_waitcnt lgkmcnt(13)\n\t"
    "v_mfma_f32_16x16x32_bf16 v[88:91], a[76:79], v[104:107], v[88:91]\n\t"
    "ds_read_b128 a[156:159], v7 offset:38912\n\t"
    "v_mfma_f32_16x16x32_bf16 v[100:103], a[4:7], v[80:83], v[100:103]\n\t"
    "ds_read_b128 a[96:99], v7 offset:39936\n\t"
    "v_mfma_f32_16x16x32_bf16 v[72:75], a[28:31], v[80:83], v[72:75]\n\t"
    "v_cvt_pk_bf16_f32 v240, v84, v85\n\t"
    "v_cvt_pk_bf16_f32 v241, v86, v87\n\t"
    "v_mfma_f32_16x16x32_bf16 v[108:111], a[76:79], v[80:83], v[108:111]\n\t"
    "ds_write_b64 v32, v[240:241] offset:0\n\t"
    "s_nop 3\n\t"
    "v_mfma_f32_16x16x32_bf16 v[92:95], a[124:127], a[0:3], 0\n\t"
    "s_waitcnt lgkmcnt(10)\n\t"
    "v_mfma_f32_16x16x32_bf16 v[84:87], a[124:127], a[24:27], 0\n\t"
    "ds_read_b128 a[160:163], v7 offset:40960\n\t"
    "v_mfma_f32_16x16x32_bf16 v[92:95], a[100:103], a[4:7], v[92:95]\n\t"
    "s_nop 3\n\t"
    "v_mfma_f32_16x16x32_bf16 v[84:87], a[100:103], a[28:31], v[84:87]\n\t"
    "ds_read_b128 a[124:127], v7 offset:41984\n\t"
    "s_waitcnt lgkmcnt(8)\n\t"
    "v_mfma_f32_16x16x32_bf16 v[92:95], a[128:131], a[8:11], v[92:95]\n\t"
    "v_mfma_f32_16x16x32_bf16 v[84:87], a[128:131], a[32:35], v[84:87]\n\t"
    "ds_read_b128 a[100:103], v7 offset:43008\n\t"
    "s_nop 3\n\t"
    "v_mfma_f32_16x16x32_bf16 v[92:95], a[104:107], a[12:15], v[92:95]\n\t"
    "s_waitcnt lgkmcnt(5)\n\t"
    "v_mfma_f32_16x16x32_bf16 v[84:87], a[104:107], a[36:39], v[84:87]\n\t"
    "ds_read_b128 a[128:131], v7 offset:44032\n\t"
    "v_mfma_f32_16x16x32_bf16 v[92:95], a[144:147], a[16:19], v[92:95]\n\t"
    "s_nop 3\n\t"
    "v_mfma_f32_16x16x32_bf16 v[84:87], a[144:147], a[40:43], v[84:87]\n\t"
    "ds_read_b128 a[104:107], v7 offset:45056\n\t"
    "v_mfma_f32_16x16x32_bf16 v[92:95], a[132:135], a[20:23], v[92:95]\n\t"
    "v_mfma_f32_16x16x32_bf16 v[84:87], a[132:135], a[44:47], v[84:87]\n\t"
    "ds_read_b128 a[164:167], v7 offset:46080\n\t"
    "s_nop 3\n\t"
    "v_mfma_f32_16x16x32_bf16 v[96:99], a[148:151], a[0:3], 0\n\t"
    "v_mfma_f32_16x16x32_bf16 v[112:115], a[148:151], a[24:27], 0\n\t"
    "v_cvt_pk_bf16_f32 v116, v92, v93\n\t"
    "v_cvt_pk_bf16_f32 v117, v94, v95\n\t"
    "v_mfma_f32_16x16x32_bf16 v[96:99], a[108:111], a[4:7], v[96:99]\n\t"
    "v_cvt_pk_bf16_f32 v120, v84, v85\n\t"
    "v_cvt_pk_bf16_f32 v121, v86, v87\n\t"
    "s_nop 3\n\t"
    "v_mfma_f32_16x16x32_bf16 v[112:115], a[108:111], a[28:31], v[112:115]\n\t"
    "ds_read_b128 a[132:135], v7 offset:47104\n\t"
    "ds_read_b128 a[144:147], v7 offset:48128\n\t"
    "v_mfma_f32_16x16x32_bf16 v[96:99], a[112:115], a[8:11], v[96:99]\n\t"
    "s_waitcnt lgkmcnt(5)\n\t"
    "v_mfma_f32_16x16x32_bf16 v[112:115], a[112:115], a[32:35], v[112:115]\n\t"
    "s_nop 3\n\t"
    "v_mfma_f32_16x16x32_bf16 v[96:99], a[116:119], a[12:15], v[96:99]\n\t"
    "v_mfma_f32_16x16x32_bf16 v[112:115], a[116:119], a[36:39], v[112:115]\n\t"
    "v_mfma_f32_16x16x32_bf16 v[96:99], a[120:123], a[16:19], v[96:99]\n\t"
    "s_nop 3\n\t"
    "v_mfma_f32_16x16x32_bf16 v[112:115], a[120:123], a[40:43], v[112:115]\n\t"
    "v_mfma_f32_16x16x32_bf16 v[96:99], a[136:139], a[20:23], v[96:99]\n\t"
    "v_mfma_f32_16x16x32_bf16 v[112:115], a[136:139], a[44:47], v[112:115]\n\t"
    "s_waitcnt vmcnt(6)\n\t"
    "s_barrier\n\t"
    "s_nop 3\n\t"
    "v_mfma_f32_16x16x32_bf16 v[84:87], a[152:155], a[0:3], 0\n\t"
    "v_cvt_pk_bf16_f32 v118, v96, v97\n\t"
    "v_cvt_pk_bf16_f32 v119, v98, v99\n\t"
    "v_mfma_f32_16x16x32_bf16 v[92:95], a[152:155], a[24:27], 0\n\t"
    "v_cvt_pk_bf16_f32 v122, v112, v113\n\t"
    "v_cvt_pk_bf16_f32 v123, v114, v115\n\t"
    "v_mfma_f32_16x16x32_bf16 v[44:47], a[8:11], v[116:119], v[44:47]\n\t"
    "ds_read_b128 a[108:111], v7 offset:49152\n\t"
    "ds_read_b128 a[112:115], v7 offset:50176\n\t"
    "s_nop 3\n\t"
    "v_mfma_f32_16x16x32_bf16 v[68:71], a[32:35], v[116:119], v[68:71]\n\t"
    "ds_read_b128 a[116:119], v7 offset:51200\n\t"
    "s_waitcnt lgkmcnt(3)\n\t"
    "v_mfma_f32_16x16x32_bf16 v[88:91], a[80:83], v[116:119], v[88:91]\n\t"
    "ds_read_b128 a[120:123], v7 offset:52224\n\t"
    "v_mfma_f32_16x16x32_bf16 v[100:103], a[8:11], v[120:123], v[100:103]\n\t"
    "ds_read_b128 a[136:139], v7 offset:53248\n\t"
    "s_nop 3\n\t"
    "v_mfma_f32_16x16x32_bf16 v[72:75], a[32:35], v[120:123], v[72:75]\n\t"
    "v_mfma_f32_16x16x32_bf16 v[108:111], a[80:83], v[120:123], v[108:111]\n\t"
    "v_mfma_f32_16x16x32_bf16 v[84:87], a[140:143], a[4:7], v[84:87]\n\t"
    "s_nop 3\n\t"
    "v_mfma_f32_16x16x32_bf16 v[92:95], a[140:143], a[28:31], v[92:95]\n\t"
    "ds_read_b128 a[148:151], v7 offset:54272\n\t"
    "v_mfma_f32_16x16x32_bf16 v[84:87], a[156:159], a[8:11], v[84:87]\n\t"
    "v_mfma_f32_16x16x32_bf16 v[92:95], a[156:159], a[32:35], v[92:95]\n\t"
    "ds_read_b128 a[152:155], v7 offset:55296\n\t"
    "s_nop 3\n\t"
    "v_mfma_f32_16x16x32_bf16 v[84:87], a[96:99], a[12:15], v[84:87]\n\t"
    "v_mfma_f32_16x16x32_bf16 v[92:95], a[96:99], a[36:39], v[92:95]\n\t"
    "ds_read_b128 a[140:143], v7 offset:56320\n\t"
    "v_mfma_f32_16x16x32_bf16 v[84:87], a[160:163], a[16:19], v[84:87]\n\t"
    "s_nop 3\n\t"
    "v_mfma_f32_16x16x32_bf16 v[92:95], a[160:163], a[40:43], v[92:95]\n\t"
    "ds_read_b128 a[96:99], v7 offset:57344\n\t"
    "s_waitcnt lgkmcnt(4)\n\t"
    "v_mfma_f32_16x16x32_bf16 v[84:87], a[124:127], a[20:23], v[84:87]\n\t"
    "v_mfma_f32_16x16x32_bf16 v[92:95], a[124:127], a[44:47], v[92:95]\n\t"
    "ds_read_b128 a[156:159], v7 offset:58368\n\t"
    "s_nop 3\n\t"
    "v_mfma_f32_16x16x32_bf16 v[96:99], a[100:103], a[0:3], 0\n\t"
    "buffer_load_dwordx4 v[112:115], v0, s[16:19], s61 offen sc1\n\t"
    "v_mfma_f32_16x16x32_bf16 v[124:127], a[100:103], a[24:27], 0\n\t"
    "v_cvt_pk_bf16_f32 v128, v84, v85\n\t"
    "v_cvt_pk_bf16_f32 v129, v86, v87\n\t"
    "v_mfma_f32_16x16x32_bf16 v[96:99], a[128:131], a[4:7], v[96:99]\n\t"
    "v_cvt_pk_bf16_f32 v132, v92, v93\n\t"
    "v_cvt_pk_bf16_f32 v133, v94, v95\n\t"
    "s_nop 3\n\t"
    "v_mfma_f32_16x16x32_bf16 v[124:127], a[128:131], a[28:31], v[124:127]\n\t"
    "ds_read_b128 a[100:103], v7 offset:59392\n\t"
    "ds_read_b128 a[124:127], v7 offset:60416\n\t"
    "v_mfma_f32_16x16x32_bf16 v[96:99], a[104:107], a[8:11], v[96:99]\n\t"
    "v_mfma_f32_16x16x32_bf16 v[124:127], a[104:107], a[32:35], v[124:127]\n\t"
    "ds_read_b128 a[128:131], v7 offset:61440\n\t"
    "s_nop 3\n\t"
    "v_mfma_f32_16x16x32_bf16 v[96:99], a[164:167], a[12:15], v[96:99]\n\t"
    "v_mfma_f32_16x16x32_bf16 v[124:127], a[164:167], a[36:39], v[124:127]\n\t"
    "ds_read_b128 a[104:107], v7 offset:62464\n\t"
    "s_waitcnt lgkmcnt(5)\n\t"
    "v_mfma_f32_16x16x32_bf16 v[96:99], a[132:135], a[16:19], v[96:99]\n\t"
    "s_nop 3\n\t"
    "v_mfma_f32_16x16x32_bf16 v[124:127], a[132:135], a[40:43], v[124:127]\n\t"
    "ds_read_b128 a[160:163], v7 offset:63488\n\t"
    "v_mfma_f32_16x16x32_bf16 v[96:99], a[144:147], a[20:23], v[96:99]\n\t"
    "v_mfma_f32_16x16x32_bf16 v[124:127], a[144:147], a[44:47], v[124:127]\n\t"
    "ds_read_b128 a[164:167], v7 offset:64512\n\t"
    "s_nop 3\n\t"
    "v_mfma_f32_16x16x32_bf16 v[84:87], a[108:111], a[0:3], 0\n\t"
    "v_mfma_f32_16x16x32_bf16 v[92:95], a[108:111], a[24:27], 0\n\t"
    "v_cvt_pk_bf16_f32 v130, v96, v97\n\t"
    "v_cvt_pk_bf16_f32 v131, v98, v99\n\t"
    "v_mfma_f32_16x16x32_bf16 v[84:87], a[112:115], a[4:7], v[84:87]\n\t"
    "v_cvt_pk_bf16_f32 v134, v124, v125\n\t"
    "v_cvt_pk_bf16_f32 v135, v126, v127\n\t"
    "s_nop 3\n\t"
    "v_mfma_f32_16x16x32_bf16 v[44:47], a[12:15], v[128:131], v[44:47]\n\t"
    "ds_read_b128 a[108:111], v8 offset:0\n\t"
    "s_waitcnt lgkmcnt(4)\n\t"
    "v_mfma_f32_16x16x32_bf16 v[68:71], a[36:39], v[128:131], v[68:71]\n\t"
    "v_mfma_f32_16x16x32_bf16 v[88:91], a[84:87], v[128:131], v[88:91]\n\t"
    "s_nop 3\n\t"
    "v_mfma_f32_16x16x32_bf16 v[100:103], a[12:15], v[132:135], v[100:103]\n\t"
    "v_mfma_f32_16x16x32_bf16 v[72:75], a[36:39], v[132:135], v[72:75]\n\t"
    "v_mfma_f32_16x16x32_bf16 v[108:111], a[84:87], v[132:135], v[108:111]\n\t"
    "s_nop 3\n\t"
    "v_mfma_f32_16x16x32_bf16 v[92:95], a[112:115], a[28:31], v[92:95]\n\t"
    "ds_read_b128 a[132:135], v8 offset:1024\n\t"
    "v_mfma_f32_16x16x32_bf16 v[84:87], a[116:119], a[8:11], v[84:87]\n\t"
    "v_mfma_f32_16x16x32_bf16 v[92:95], a[116:119], a[32:35], v[92:95]\n\t"
    "ds_read_b128 a[144:147], v8 offset:2048\n\t"
    "s_nop 3\n\t"
    "v_mfma_f32_16x16x32_bf16 v[84:87], a[120:123], a[12:15], v[84:87]\n\t"
    "v_mfma_f32_16x16x32_bf16 v[92:95], a[120:123], a[36:39], v[92:95]\n\t"
    "ds_read_b128 a[112:115], v8 offset:3072\n\t"
    "v_mfma_f32_16x16x32_bf16 v[84:87], a[136:139], a[16:19], v[84:87]\n\t"
    "s_waitcnt lgkmcnt(3)\n\t"
    "s_nop 3\n\t"
    "v_mfma_f32_16x16x32_bf16 v[92:95], a[136:139], a[40:43], v[92:95]\n\t"
    "ds_read_b128 a[116:119], v8 offset:4096\n\t"
    "v_mfma_f32_16x16x32_bf16 v[84:87], a[148:151], a[20:23], v[84:87]\n\t"
    "v_mfma_f32_16x16x32_bf16 v[92:95], a[148:151], a[44:47], v[92:95]\n\t"
    "ds_read_b128 a[120:123], v8 offset:5120\n\t"
    "s_nop 3\n\t"
    "v_mfma_f32_16x16x32_bf16 v[96:99], a[152:155], a[0:3], 0\n\t"
    "v_mfma_f32_16x16x32_bf16 v[124:127], a[152:155], a[24:27], 0\n\t"
    "v_cvt_pk_bf16_f32 v136, v84, v85\n\t"
    "v_cvt_pk_bf16_f32 v137, v86, v87\n\t"
    "v_mfma_f32_16x16x32_bf16 v[96:99], a[140:143], a[4:7], v[96:99]\n\t"
    "v_cvt_pk_bf16_f32 v140, v92, v93\n\t"
    "v_cvt_pk_bf16_f32 v141, v94, v95\n\t"
    "s_nop 3\n\t"
    "v_mfma_f32_16x16x32_bf16 v[124:127], a[140:143], a[28:31], v[124:127]\n\t"
    "ds_read_b128 a[136:139], v8 offset:6144\n\t"
    "ds_read_b128 a[148:151], v8 offset:7168\n\t"
    "v_mfma_f32_16x16x32_bf16 v[96:99], a[96:99], a[8:11], v[96:99]\n\t"
    "v_mfma_f32_16x16x32_bf16 v[124:127], a[96:99], a[32:35], v[124:127]\n\t"
    "ds_read_b128 a[140:143], v7 offset:0\n\t"
    "s_nop 3\n\t"
    "v_mfma_f32_16x16x32_bf16 v[96:99], a[156:159], a[12:15], v[96:99]\n\t"
    "ds_read_b128 a[152:155], v7 offset:1024\n\t"
    "v_mfma_f32_16x16x32_bf16 v[124:127], a[156:159], a[36:39], v[124:127]\n\t"
    "ds_read_b128 a[96:99], v7 offset:2048\n\t"
    "s_waitcnt lgkmcnt(6)\n\t"
    "v_mfma_f32_16x16x32_bf16 v[96:99], a[100:103], a[16:19], v[96:99]\n\t"
    "ds_read_b128 a[168:171], v7 offset:3072\n\t"
    "s_nop 3\n\t"
    "v_mfma_f32_16x16x32_bf16 v[124:127], a[100:103], a[40:43], v[124:127]\n\t"
    "ds_read_b128 a[156:159], v7 offset:4096\n\t"
    "v_mfma_f32_16x16x32_bf16 v[96:99], a[124:127], a[20:23], v[96:99]\n\t"
    "ds_read_b128 a[172:175], v7 offset:5120\n\t"
    "v_mfma_f32_16x16x32_bf16 v[124:127], a[124:127], a[44:47], v[124:127]\n\t"
    "ds_read_b128 a[100:103], v7 offset:6144\n\t"
    "s_nop 3\n\t"
    "v_mfma_f32_16x16x32_bf16 v[84:87], a[128:131], a[0:3], 0\n\t"
    "v_cvt_pk_bf16_f32 v138, v96, v97\n\t"
    "v_cvt_pk_bf16_f32 v139, v98, v99\n\t"
    "v_mfma_f32_16x16x32_bf16 v[92:95], a[128:131], a[24:27], 0\n\t"
    "v_cvt_pk_bf16_f32 v142, v124, v125\n\t"
    "v_cvt_pk_bf16_f32 v143, v126, v127\n\t"
    "v_mfma_f32_16x16x32_bf16 v[44:47], a[16:19], v[136:139], v[44:47]\n\t"
    "ds_read_b128 a[124:127], v7 offset:7168\n\t"
    "ds_read_b128 a[128:131], v7 offset:8192\n\t"
    "s_nop 3\n\t"
    "v_mfma_f32_16x16x32_bf16 v[68:71], a[40:43], v[136:139], v[68:71]\n\t"
    "s_waitcnt lgkmcnt(8)\n\t"
    "v_mfma_f32_16x16x32_bf16 v[88:91], a[88:91], v[136:139], v[88:91]\n\t"
    "v_mfma_f32_16x16x32_bf16 v[100:103], a[16:19], v[140:143], v[100:103]\n\t"
    "s_nop 3\n\t"
    "v_mfma_f32_16x16x32_bf16 v[72:75], a[40:43], v[140:143], v[72:75]\n\t"
    "v_mfma_f32_16x16x32_bf16 v[108:111], a[88:91], v[140:143], v[108:111]\n\t"
    "v_mfma_f32_16x16x32_bf16 v[84:87], a[104:107], a[4:7], v[84:87]\n\t"
    "ds_read_b128 a[176:179], v7 offset:9216\n\t"
    "s_waitcnt lgkmcnt(4)\n\t"
    "s_nop 3\n\t"
    "v_mfma_f32_16x16x32_bf16 v[92:95], a[104:107], a[28:31], v[92:95]\n\t"
    "ds_read_b128 a[180:183], v7 offset:10240\n\t"
    "v_mfma_f32_16x16x32_bf16 v[84:87], a[160:163], a[8:11], v[84:87]\n\t"
    "ds_read_b128 a[184:187], v7 offset:11264\n\t"
    "v_mfma_f32_16x16x32_bf16 v[92:95], a[160:163], a[32:35], v[92:95]\n\t"
    "ds_read_b128 a[104:107], v7 offset:12288\n\t"
    "s_nop 3\n\t"
    "v_mfma_f32_16x16x32_bf16 v[84:87], a[164:167], a[12:15], v[84:87]\n\t"
    "ds_read_b128 a[188:191], v7 offset:13312\n\t"
    "s_waitcnt vmcnt(0)\n\t"
    "v_mfma_f32_16x16x32_bf16 v[92:95], a[164:167], a[36:39], v[92:95]\n\t"
    "v_cmp_eq_u64 s[46:47], s[44:45], v[112:113]\n\t"
    "v_cmp_eq_u64 s[62:63], s[44:45], v[114:115]\n\t"
    "v_mfma_f32_16x16x32_bf16 v[84:87], a[108:111], a[16:19], v[84:87]\n\t"
    "ds_read_b128 a[48:51], v10 offset:12288\n\t"
    "s_and_b64 s[46:47], s[46:47], s[62:63]\n\t"
    "s_nop 3\n\t"
    "v_mfma_f32_16x16x32_bf16 v[92:95], a[108:111], a[40:43], v[92:95]\n\t"
    "ds_read_b128 a[52:55], v11 offset:12288\n\t"
    "v_mfma_f32_16x16x32_bf16 v[84:87], a[132:135], a[20:23], v[84:87]\n\t"
    "ds_read_b128 a[56:59], v10 offset:13312\n\t"
    "v_mfma_f32_16x16x32_bf16 v[92:95], a[132:135], a[44:47], v[92:95]\n\t"
    "s_andn2_b64 s[46:47], s[56:57], s[46:47]\n\t"
    "s_cbranch_scc1 Lfslow%=\n\t"
    "Lfback%=:\n\t"
    "; keep v[112:115]\n\t"
    "s_nop 3\n\t"
    "v_mfma_f32_16x16x32_bf16 v[96:99], a[144:147], a[0:3], 0\n\t"
    "ds_read_b128 a[60:63], v11 offset:13312\n\t"
    "s_add_u32 m0, s20, 0\n\t"
    "v_mfma_f32_16x16x32_bf16 v[112:115], a[144:147], a[24:27], 0\n\t"
    "ds_read_b128 a[64:67], v10 offset:14336\n\t"
    "v_cvt_pk_bf16_f32 v124, v84, v85\n\t"
    "v_mfma_f32_16x16x32_bf16 v[96:99], a[112:115], a[4:7], v[96:99]\n\t"
    "ds_read_b128 a[68:71], v11 offset:14336\n\t"
    "v_cvt_pk_bf16_f32 v125, v86, v87\n\t"
    "s_nop 3\n\t"
    "v_mfma_f32_16x16x32_bf16 v[112:115], a[112:115], a[28:31], v[112:115]\n\t"
    "buffer_load_dwordx4 v12, s[16:19], s27 offen offset:0 sc1 lds\n\t"
    "s_waitcnt lgkmcnt(5)\n\t"
    "v_mfma_f32_16x16x32_bf16 v[84:87], a[48:51], v[76:79], 0\n\t"
    "v_cvt_pk_bf16_f32 v144, v92, v93\n\t"
    "v_cvt_pk_bf16_f32 v145, v94, v95\n\t"
    "v_mfma_f32_16x16x32_bf16 v[148:151], a[48:51], v[40:43], 0\n\t"
    "ds_read_b128 a[108:111], v7 offset:14336\n\t"
    "ds_read_b128 a[112:115], v7 offset:15360\n\t"
    "s_nop 3\n\t"
    "s_waitcnt lgkmcnt(6)\n\t"
    "v_mfma_f32_16x16x32_bf16 v[84:87], a[52:55], v[104:107], v[84:87]\n\t"
    "ds_read_b128 a[132:135], v7 offset:16384\n\t"
    "v_mfma_f32_16x16x32_bf16 v[148:151], a[52:55], v[80:83], v[148:151]\n\t"
    "ds_read_b128 a[144:147], v7 offset:17408\n\t"
    "s_waitcnt lgkmcnt(7)\n\t"
    "v_mfma_f32_16x16x32_bf16 v[84:87], a[56:59], v[116:119], v[84:87]\n\t"
    "buffer_load_dwordx4 v12, s[16:19], s27 offen offset:1024 sc1 lds\n\t"
    "s_nop 3\n\t"
    "v_mfma_f32_16x16x32_bf16 v[148:151], a[56:59], v[120:123], v[148:151]\n\t"
    "ds_read_b128 a[160:163], v7 offset:18432\n\t"
    "s_waitcnt lgkmcnt(7)\n\t"
    "Lst1%=:\n\t"
    "s_cbranch_execz Lst2%=\n\t"
    "v_mfma_f32_16x16x32_bf16 v[84:87], a[60:63], v[128:131], v[84:87]\n\t"
    "ds_read_b128 a[164:167], v7 offset:19456\n\t"
    "v_mfma_f32_16x16x32_bf16 v[148:151], a[60:63], v[132:135], v[148:151]\n\t"
    "ds_read_b128 a[192:195], v7 offset:20480\n\t"
    "s_nop 3\n\t"
    "s_waitcnt lgkmcnt(7)\n\t"
    "v_mfma_f32_16x16x32_bf16 v[84:87], a[64:67], v[136:139], v[84:87]\n\t"
    "ds_read_b128 a[196:199], v7 offset:21504\n\t"
    "v_mfma_f32_16x16x32_bf16 v[148:151], a[64:67], v[140:143], v[148:151]\n\t"
    "buffer_load_dwordx4 v12, s[16:19], s27 offen offset:2048 sc1 lds\n\t"
    "v_mfma_f32_16x16x32_bf16 v[96:99], a[116:119], a[8:11], v[96:99]\n\t"
    "ds_read_b128 a[200:203], v7 offset:22528\n\t"
    "s_nop 3\n\t"
    "v_mfma_f32_16x16x32_bf16 v[112:115], a[116:119], a[32:35], v[112:115]\n\t"
    "ds_read_b128 a[204:207], v7 offset:23552\n\t"
    "v_mfma_f32_16x16x32_bf16 v[96:99], a[120:123], a[12:15], v[96:99]\n\t"
    "ds_read_b128 a[208:211], v7 offset:24576\n\t"
    "v_mfma_f32_16x16x32_bf16 v[112:115], a[120:123], a[36:39], v[112:115]\n\t"
    "ds_read_b128 a[116:119], v7 offset:25600\n\t"
    "s_waitcnt lgkmcnt(8)\n\t"
    "s_nop 3\n\t"
    "v_mfma_f32_16x16x32_bf16 v[96:99], a[136:139], a[16:19], v[96:99]\n\t"
    "buffer_load_dwordx4 v12, s[16:19], s27 offen offset:3072 sc1 lds\n\t"
    "v_mfma_f32_16x16x32_bf16 v[112:115], a[136:139], a[40:43], v[112:115]\n\t"
    "ds_read_b128 a[120:123], v7 offset:26624\n\t"
    "s_add_u32 m0, s20, 4096\n\t"
    "v_mfma_f32_16x16x32_bf16 v[96:99], a[148:151], a[20:23], v[96:99]\n\t"
    "ds_read_b128 a[212:215], v7 offset:27648\n\t"
    "s_nop 3\n\t"
    "v_mfma_f32_16x16x32_bf16 v[112:115], a[148:151], a[44:47], v[112:115]\n\t"
    "ds_read_b128 a[136:139], v7 offset:28672\n\t"
    "v_mfma_f32_16x16x32_bf16 v[40:43], a[140:143], a[48:51], 0\n\t"
    "v_cvt_pk_bf16_f32 v126, v96, v97\n\t"
    "v_cvt_pk_bf16_f32 v127, v98, v99\n\t"
    "v_mfma_f32_16x16x32_bf16 v[40:43], a[152:155], a[52:55], v[40:43]\n\t"
    "buffer_load_dwordx4 v12, s[16:19], s28 offen offset:0 sc1 lds\n\t"
    "s_nop 3\n\t"
    "v_mfma_f32_16x16x32_bf16 v[44:47], a[20:23], v[124:127], v[44:47]\n\t"
    "v_cvt_pk_bf16_f32 v146, v112, v113\n\t"
    "v_cvt_pk_bf16_f32 v147, v114, v115\n\t"
    "v_mfma_f32_16x16x32_bf16 v[40:43], a[96:99], a[56:59], v[40:43]\n\t"
    "ds_read_b128 a[140:143], v7 offset:29696\n\t"
    "ds_read_b128 a[148:151], v7 offset:30720\n\t"
    "v_mfma_f32_16x16x32_bf16 v[68:71], a[44:47], v[124:127], v[68:71]\n\t"
    "ds_read_b128 a[152:155], v7 offset:31744\n\t"
    "s_waitcnt lgkmcnt(7)\n\t"
    "s_nop 3\n\t"
    "v_mfma_f32_16x16x32_bf16 v[40:43], a[168:171], a[60:63], v[40:43]\n\t"
    "ds_read_b128 a[96:99], v7 offset:32768\n\t"
    "v_max3_f32 v242, v44, v45, v46\n\t"
    "v_mfma_f32_16x16x32_bf16 v[84:87], a[68:71], v[124:127], v[84:87]\n\t"
    "buffer_load_dwordx4 v12, s[16:19], s28 offen offset:1024 sc1 lds\n\t"
    "v_mfma_f32_16x16x32_bf16 v[40:43], a[156:159], a[64:67], v[40:43]\n\t"
    "ds_read_b128 a[168:171], v7 offset:33792\n\t"
    "v_max3_f32 v242, v242, v47, v68\n\t"
    "v_mfma_f32_16x16x32_bf16 v[88:91], a[92:95], v[124:127], v[88:91]\n\t"
    "ds_read_b128 a[216:219], v7 offset:34816\n\t"
    "v_max3_f32 v242, v242, v69, v70\n\t"
    "v_mfma_f32_16x16x32_bf16 v[40:43], a[172:175], a[68:71], v[40:43]\n\t"
    "ds_read_b128 a[156:159], v7 offset:35840\n\t"
    "v_max3_f32 v242, v242, v71, v84\n\t"
    "v_mfma_f32_16x16x32_bf16 v[100:103], a[20:23], v[144:147], v[100:103]\n\t"
    "ds_read_b128 a[220:223], v7 offset:36864\n\t"
    "v_max3_f32 v242, v242, v85, v86\n\t"
    "v_mfma_f32_16x16x32_bf16 v[76:79], a[100:103], a[48:51], 0\n\t"
    "buffer_load_dwordx4 v12, s[16:19], s28 offen offset:2048 sc1 lds\n\t"
    "v_mfma_f32_16x16x32_bf16 v[72:75], a[44:47], v[144:147], v[72:75]\n\t"
    "v_cvt_pk_bf16_f32 v80, v40, v41\n\t"
    "v_cvt_pk_bf16_f32 v81, v42, v43\n\t"
    "v_mfma_f32_16x16x32_bf16 v[76:79], a[124:127], a[52:55], v[76:79]\n\t"
    "v_cndmask_b32_e64 v241, v35, v88, s[22:23]\n\t"
    "v_max3_f32 v242, v242, v87, v241\n\t"
    "v_mfma_f32_16x16x32_bf16 v[148:151], a[68:71], v[144:147], v[148:151]\n\t"
    "ds_read_b128 a[100:103], v7 offset:37888\n\t"
    "v_max3_f32 v240, v100, v101, v102\n\t"
    "v_mfma_f32_16x16x32_bf16 v[76:79], a[128:131], a[56:59], v[76:79]\n\t"
    "ds_read_b128 a[124:127], v7 offset:38912\n\t"
    "v_max3_f32 v240, v240, v103, v72\n\t"
    "v_mfma_f32_16x16x32_bf16 v[108:111], a[92:95], v[144:147], v[108:111]\n\t"
    "buffer_load_dwordx4 v12, s[16:19], s28 offen offset:3072 sc1 lds\n\t"
    "v_mfma_f32_16x16x32_bf16 v[76:79], a[176:179], a[60:63], v[76:79]\n\t"
    "s_add_u32 m0, s20, 8192\n\t"
    "v_max3_f32 v240, v240, v73, v74\n\t"
    "v_mfma_f32_16x16x32_bf16 v[76:79], a[180:183], a[64:67], v[76:79]\n\t"
    "ds_read_b128 a[128:131], v7 offset:39936\n\t"
    "v_max3_f32 v240, v240, v75, v148\n\t"
    "v_mfma_f32_16x16x32_bf16 v[76:79], a[184:187], a[68:71], v[76:79]\n\t"
    "v_cndmask_b32_e64 v239, v35, v108, s[22:23]\n\t"
    "v_max3_f32 v240, v240, v149, v150\n\t"
    "v_mfma_f32_16x16x32_bf16 v[40:43], a[104:107], a[48:51], 0\n\t"
    "ds_read_b128 a[172:175], v7 offset:40960\n\t"
    "v_max3_f32 v240, v240, v151, v239\n\t"
    "v_mfma_f32_16x16x32_bf16 v[40:43], a[188:191], a[52:55], v[40:43]\n\t"
    "buffer_load_dwordx4 v12, s[16:19], s29 offen offset:0 sc1 lds\n\t"
    "v_mfma_f32_16x16x32_bf16 v[40:43], a[108:111], a[56:59], v[40:43]\n\t"
    "v_cvt_pk_bf16_f32 v82, v76, v77\n\t"
    "v_cvt_pk_bf16_f32 v83, v78, v79\n\t"
    "v_mfma_f32_16x16x32_bf16 v[40:43], a[112:115], a[60:63], v[40:43]\n\t"
    "v_permlane32_swap_b32 v242, v240\n\t"
    "v_mfma_f32_16x16x32_bf16 v[88:91], a[0:3], v[80:83], 0\n\t"
    "ds_read_b128 a[104:107], v7 offset:41984\n\t"
    "v_max_f32 v242, v242, v240\n\t"
    "v_mfma_f32_16x16x32_bf16 v[40:43], a[132:135], a[64:67], v[40:43]\n\t"
    "ds_read_b128 a[108:111], v7 offset:43008\n\t"
    "v_mov_b32 v240, v242\n\t"
    "v_mfma_f32_16x16x32_bf16 v[76:79], a[24:27], v[80:83], 0\n\t"
    "buffer_load_dwordx4 v12, s[16:19], s29 offen offset:1024 sc1 lds\n\t"
    "v_mfma_f32_16x16x32_bf16 v[40:43], a[144:147], a[68:71], v[40:43]\n\t"
    "v_permlane16_swap_b32 v242, v240\n\t"
    "v_mfma_f32_16x16x32_bf16 v[92:95], a[48:51], v[80:83], 0\n\t"
    "ds_read_b128 a[112:115], v7 offset:44032\n\t"
    "v_max_f32 v242, v242, v240\n\t"
    "v_mfma_f32_16x16x32_bf16 v[96:99], a[160:163], a[48:51], 0\n\t"
    "ds_read_b128 a[132:135], v7 offset:45056\n\t"
    "v_mov_b32 v240, v242\n\t"
    "v_mfma_f32_16x16x32_bf16 v[104:107], a[72:75], v[80:83], 0\n\t"
    "v_cvt_pk_bf16_f32 v108, v40, v41\n\t"
    "v_cvt_pk_bf16_f32 v109, v42, v43\n\t"
    "v_mfma_f32_16x16x32_bf16 v[96:99], a[164:167], a[52:55], v[96:99]\n\t"
    "buffer_load_dwordx4 v12, s[16:19], s29 offen offset:2048 sc1 lds\n\t"
    "v_mfma_f32_16x16x32_bf16 v[96:99], a[192:195], a[56:59], v[96:99]\n\t"
    "v_permlane32_swap_b32 v242, v240\n\t"
    "v_mfma_f32_16x16x32_bf16 v[96:99], a[196:199], a[60:63], v[96:99]\n\t"
    "v_sub_f32 v44, v44, v242\n\t"
    "v_sub_f32 v45, v45, v242\n\t"
    "v_mfma_f32_16x16x32_bf16 v[96:99], a[200:203], a[64:67], v[96:99]\n\t"
    "v_exp_f32 v44, v44\n\t"
    "v_mfma_f32_16x16x32_bf16 v[96:99], a[204:207], a[68:71], v[96:99]\n\t"
    "v_exp_f32 v45, v45\n\t"
    "v_mfma_f32_16x16x32_bf16 v[40:43], a[208:211], a[48:51], 0\n\t"
    "ds_read_b128 a[144:147], v7 offset:46080\n\t"
    "s_waitcnt lgkmcnt(5)\n\t"
    "v_mfma_f32_16x16x32_bf16 v[40:43], a[116:119], a[52:55], v[40:43]\n\t"
    "buffer_load_dwordx4 v12, s[16:19], s29 offen offset:3072 sc1 lds\n\t"
    "v_mfma_f32_16x16x32_bf16 v[40:43], a[120:123], a[56:59], v[40:43]\n\t"
    "ds_read_b128 a[160:163], v7 offset:47104\n\t"
    "s_add_u32 m0, s20, 12288\n\t"
    "v_mfma_f32_16x16x32_bf16 v[40:43], a[212:215], a[60:63], v[40:43]\n\t"
    "ds_read_b128 a[116:119], v7 offset:48128\n\t"
    "v_cvt_pk_bf16_f32 v110, v96, v97\n\t"
    "v_mfma_f32_16x16x32_bf16 v[40:43], a[136:139], a[64:67], v[40:43]\n\t"
    "ds_read_b128 a[120:123], v7 offset:49152\n\t"
    "v_cvt_pk_bf16_f32 v111, v98, v99\n\t"
    "v_mfma_f32_16x16x32_bf16 v[40:43], a[140:143], a[68:71], v[40:43]\n\t"
    "ds_read_b128 a[164:167], v7 offset:50176\n\t"
    "v_sub_f32 v46, v46, v242\n\t"
    "v_mfma_f32_16x16x32_bf16 v[88:91], a[4:7], v[108:111], v[88:91]\n\t"
    "buffer_load_dwordx4 v12, s[16:19], s30 offen offset:0 sc1 lds\n\t"
    "v_mfma_f32_16x16x32_bf16 v[80:83], a[148:151], a[48:51], 0\n\t"
    "ds_read_b128 a[136:139], v7 offset:51200\n\t"
    "v_add_f32 v238, v44, v45\n\t"
    "v_mfma_f32_16x16x32_bf16 v[76:79], a[28:31], v[108:111], v[76:79]\n\t"
    "ds_read_b128 a[140:143], v7 offset:52224\n\t"
    "v_cvt_pk_bf16_f32 v96, v40, v41\n\t"
    "v_mfma_f32_16x16x32_bf16 v[80:83], a[152:155], a[52:55], v[80:83]\n\t"
    "ds_read_b128 a[148:151], v7 offset:53248\n\t"
    "v_cvt_pk_bf16_f32 v97, v42, v43\n\t"
    "v_mfma_f32_16x16x32_bf16 v[92:95], a[52:55], v[108:111], v[92:95]\n\t"
    "v_exp_f32 v46, v46\n\t"
    "v_mfma_f32_16x16x32_bf16 v[80:83], a[96:99], a[56:59], v[80:83]\n\t"
    "buffer_load_dwordx4 v12, s[16:19], s30 offen offset:1024 sc1 lds\n\t"
    "v_mfma_f32_16x16x32_bf16 v[104:107], a[76:79], v[108:111], v[104:107]\n\t"
    "ds_read_b128 a[152:155], v7 offset:54272\n\t"
    "v_sub_f32 v47, v47, v242\n\t"
    "v_mfma_f32_16x16x32_bf16 v[80:83], a[168:171], a[60:63], v[80:83]\n\t"
    "ds_read_b128 a[96:99], v7 offset:55296\n\t"
    "ds_read_b128 a[176:179], v7 offset:56320\n\t"
    "v_mfma_f32_16x16x32_bf16 v[80:83], a[216:219], a[64:67], v[80:83]\n\t"
    "v_exp_f32 v47, v47\n\t"
    "v_mfma_f32_16x16x32_bf16 v[80:83], a[156:159], a[68:71], v[80:83]\n\t"
    "ds_read_b128 a[168:171], v7 offset:57344\n\t"
    "v_add_f32 v238, v238, v46\n\t"
    "v_mfma_f32_16x16x32_bf16 v[40:43], a[220:223], a[48:51], 0\n\t"
    "buffer_load_dwordx4 v12, s[16:19], s30 offen offset:2048 sc1 lds\n\t"
    "v_mfma_f32_16x16x32_bf16 v[40:43], a[100:103], a[52:55], v[40:43]\n\t"
    "ds_read_b128 a[156:159], v7 offset:58368\n\t"
    "v_sub_f32 v68, v68, v242\n\t"
    "v_mfma_f32_16x16x32_bf16 v[40:43], a[124:127], a[56:59], v[40:43]\n\t"
    "ds_read_b128 a[180:183], v7 offset:59392\n\t"
    "v_cvt_pk_bf16_f32 v98, v80, v81\n\t"
    "v_mfma_f32_16x16x32_bf16 v[40:43], a[128:131], a[60:63], v[40:43]\n\t"
    "ds_read_b128 a[100:103], v7 offset:60416\n\t"
    "v_cvt_pk_bf16_f32 v99, v82, v83\n\t"
    "v_mfma_f32_16x16x32_bf16 v[40:43], a[172:175], a[64:67], v[40:43]\n\t"
    "ds_read_b128 a[124:127], v7 offset:61440\n\t"
    "s_waitcnt lgkmcnt(10)\n\t"
    "v_mfma_f32_16x16x32_bf16 v[88:91], a[8:11], v[96:99], v[88:91]\n\t"
    "buffer_load_dwordx4 v12, s[16:19], s30 offen offset:3072 sc1 lds\n\t"
    "v_mfma_f32_16x16x32_bf16 v[40:43], a[104:107], a[68:71], v[40:43]\n\t"
    "ds_read_b128 a[128:131], v7 offset:62464\n\t"
    "s_add_u32 m0, s20, 16384\n\t"
    "v_mfma_f32_16x16x32_bf16 v[76:79], a[32:35], v[96:99], v[76:79]\n\t"
    "ds_read_b128 a[172:175], v7 offset:63488\n\t"
    "v_add_f32 v238, v238, v47\n\t"
    "v_mfma_f32_16x16x32_bf16 v[80:83], a[108:111], a[48:51], 0\n\t"
    "ds_read_b128 a[104:107], v7 offset:64512\n\t"
    "v_sub_f32 v69, v69, v242\n\t"
    "v_mfma_f32_16x16x32_bf16 v[92:95], a[56:59], v[96:99], v[92:95]\n\t"
    "v_cvt_pk_bf16_f32 v108, v40, v41\n\t"
    "v_cvt_pk_bf16_f32 v109, v42, v43\n\t"
    "v_mfma_f32_16x16x32_bf16 v[80:83], a[112:115], a[52:55], v[80:83]\n\t"
    "buffer_load_dwordx4 v12, s[16:19], s31 offen offset:0 sc1 lds\n\t"
    "v_mfma_f32_16x16x32_bf16 v[104:107], a[80:83], v[96:99], v[104:107]\n\t"
    "ds_read_b128 a[108:111], v8 offset:0\n\t"
    "ds_read_b128 a[184:187], v8 offset:1024\n\t"
    "v_mfma_f32_16x16x32_bf16 v[80:83], a[132:135], a[56:59], v[80:83]\n\t"
    "v_exp_f32 v68, v68\n\t"
    "v_mfma_f32_16x16x32_bf16 v[80:83], a[144:147], a[60:63], v[80:83]\n\t"
    "ds_read_b128 a[112:115], v8 offset:2048\n\t"
    "v_add_f32 v238, v238, v68\n\t"
    "v_mfma_f32_16x16x32_bf16 v[80:83], a[160:163], a[64:67], v[80:83]\n\t"
    "ds_read_b128 a[132:135], v8 offset:3072\n\t"
    "v_sub_f32 v70, v70, v242\n\t"
    "v_mfma_f32_16x16x32_bf16 v[80:83], a[116:119], a[68:71], v[80:83]\n\t"
    "buffer_load_dwordx4 v12, s[16:19], s31 offen offset:1024 sc1 lds\n\t"
    "v_mfma_f32_16x16x32_bf16 v[40:43], a[120:123], a[48:51], 0\n\t"
    "ds_read_b128 a[144:147], v8 offset:4096\n\t"
    "v_sub_f32 v71, v71, v242\n\t"
    "v_mfma_f32_16x16x32_bf16 v[40:43], a[164:167], a[52:55], v[40:43]\n\t"
    "ds_read_b128 a[116:119], v8 offset:5120\n\t"
    "v_sub_f32 v84, v84, v242\n\t"
    "v_mfma_f32_16x16x32_bf16 v[40:43], a[136:139], a[56:59], v[40:43]\n\t"
    "ds_read_b128 a[120:123], v8 offset:6144\n\t"
    "s_waitcnt lgkmcnt(10)\n\t"
    "v_mfma_f32_16x16x32_bf16 v[40:43], a[140:143], a[60:63], v[40:43]\n\t"
    "ds_read_b128 a[160:163], v8 offset:7168\n\t"
    "v_cvt_pk_bf16_f32 v110, v80, v81\n\t"
    "v_mfma_f32_16x16x32_bf16 v[40:43], a[148:151], a[64:67], v[40:43]\n\t"
    "v_cvt_pk_bf16_f32 v111, v82, v83\n\t"
    "v_sub_f32 v85, v85, v242\n\t"
    "v_mfma_f32_16x16x32_bf16 v[40:43], a[152:155], a[68:71], v[40:43]\n\t"
    "v_exp_f32 v69, v69\n\t"
    "v_mfma_f32_16x16x32_bf16 v[88:91], a[12:15], v[108:111], v[88:91]\n\t"
    "v_exp_f32 v70, v70\n\t"
    "v_mfma_f32_16x16x32_bf16 v[80:83], a[96:99], a[48:51], 0\n\t"
    "v_add_f32 v238, v238, v69\n\t"
    "v_add_f32 v238, v238, v70\n\t"
    "v_mfma_f32_16x16x32_bf16 v[76:79], a[36:39], v[108:111], v[76:79]\n\t"
    "v_cvt_pk_bf16_f32 v96, v40, v41\n\t"
    "v_cvt_pk_bf16_f32 v97, v42, v43\n\t"
    "v_mfma_f32_16x16x32_bf16 v[80:83], a[176:179], a[52:55], v[80:83]\n\t"
    "v_exp_f32 v71, v71\n\t"
    "v_mfma_f32_16x16x32_bf16 v[92:95], a[60:63], v[108:111], v[92:95]\n\t"
    "v_exp_f32 v84, v84\n\t"
    "v_mfma_f32_16x16x32_bf16 v[80:83], a[168:171], a[56:59], v[80:83]\n\t"
    "v_add_f32 v238, v238, v71\n\t"
    "v_add_f32 v238, v238, v84\n\t"
    "v_mfma_f32_16x16x32_bf16 v[104:107], a[84:87], v[108:111], v[104:107]\n\t"
    "v_exp_f32 v85, v85\n\t"
    "v_mfma_f32_16x16x32_bf16 v[80:83], a[156:159], a[60:63], v[80:83]\n\t"
    "v_sub_f32 v86, v86, v242\n\t"
    "v_add_f32 v238, v238, v85\n\t"
    "v_mfma_f32_16x16x32_bf16 v[80:83], a[180:183], a[64:67], v[80:83]\n\t"
    "v_exp_f32 v86, v86\n\t"
    "v_mfma_f32_16x16x32_bf16 v[80:83], a[100:103], a[68:71], v[80:83]\n\t"
    "s_waitcnt vmcnt(12)\n\t"
    "s_barrier\n\t"
    "v_mfma_f32_16x16x32_bf16 v[40:43], a[124:127], a[48:51], 0\n\t"
    "ds_read_b128 a[96:99], v8 offset:8192\n\t"
    "s_waitcnt lgkmcnt(1)\n\t"
    "v_mfma_f32_16x16x32_bf16 v[40:43], a[128:131], a[52:55], v[40:43]\n\t"
    "ds_read_b128 a[100:103], v8 offset:9216\n\t"
    "v_sub_f32 v87, v87, v242\n\t"
    "v_mfma_f32_16x16x32_bf16 v[40:43], a[172:175], a[56:59], v[40:43]\n\t"
    "ds_read_b128 a[124:127], v8 offset:10240\n\t"
    "v_cvt_pk_bf16_f32 v98, v80, v81\n\t"
    "v_mfma_f32_16x16x32_bf16 v[40:43], a[104:107], a[60:63], v[40:43]\n\t"
    "ds_read_b128 a[128:131], v8 offset:11264\n\t"
    "ds_read_b128 a[136:139], v13 offset:0\n\t"
    "v_mfma_f32_16x16x32_bf16 v[40:43], a[108:111], a[64:67], v[40:43]\n\t"
    "ds_read_b128 a[140:143], v13 offset:1024\n\t"
    "v_cvt_pk_bf16_f32 v99, v82, v83\n\t"
    "v_mfma_f32_16x16x32_bf16 v[40:43], a[184:187], a[68:71], v[40:43]\n\t"
    "ds_read_b128 a[104:107], v13 offset:2048\n\t"
    "v_add_f32 v238, v238, v86\n\t"
    "v_mfma_f32_16x16x32_bf16 v[88:91], a[16:19], v[96:99], v[88:91]\n\t"
    "v_exp_f32 v87, v87\n\t"
    "v_mfma_f32_16x16x32_bf16 v[80:83], a[112:115], a[48:51], 0\n\t"
    "ds_read_b128 a[108:111], v13 offset:3072\n\t"
    "v_sub_f32 v241, v241, v242\n\t"
    "v_mfma_f32_16x16x32_bf16 v[76:79], a[40:43], v[96:99], v[76:79]\n\t"
    "v_cvt_pk_bf16_f32 v108, v40, v41\n\t"
    "v_cvt_pk_bf16_f32 v109, v42, v43\n\t"
    "v_mfma_f32_16x16x32_bf16 v[80:83], a[132:135], a[52:55], v[80:83]\n\t"
    "ds_read_b128 a[112:115], v8 offset:12288\n\t"
    "ds_read_b128 a[148:151], v8 offset:13312\n\t"
    "v_mfma_f32_16x16x32_bf16 v[92:95], a[64:67], v[96:99], v[92:95]\n\t"
    "v_exp_f32 v241, v241\n\t"
    "v_mfma_f32_16x16x32_bf16 v[80:83], a[144:147], a[56:59], v[80:83]\n\t"
    "v_add_f32 v238, v238, v87\n\t"
    "v_add_f32 v238, v238, v241\n\t"
    "v_mfma_f32_16x16x32_bf16 v[104:107], a[88:91], v[96:99], v[104:107]\n\t"
    "v_sub_f32 v100, v100, v240\n\t"
    "v_sub_f32 v101, v101, v240\n\t"
    "v_mfma_f32_16x16x32_bf16 v[80:83], a[116:119], a[60:63], v[80:83]\n\t"
    "v_exp_f32 v100, v100\n\t"
    "v_mfma_f32_16x16x32_bf16 v[80:83], a[120:123], a[64:67], v[80:83]\n\t"
    "ds_read_b128 a[132:135], v13 offset:4096\n\t"
    "ds_read_b128 a[144:147], v13 offset:5120\n\t"
    "v_mfma_f32_16x16x32_bf16 v[80:83], a[160:163], a[68:71], v[80:83]\n\t"
    "v_exp_f32 v101, v101\n\t"
    "s_waitcnt lgkmcnt(8)\n\t"
    "v_mfma_f32_16x16x32_bf16 v[40:43], a[0:3], a[96:99], 0\n\t"
    "buffer_load_dwordx4 v[96:99], v1, s[16:19], s60 offen offset:0 sc1\n\t"
    "v_mfma_f32_16x16x32_bf16 v[112:115], a[24:27], a[96:99], 0\n\t"
    "buffer_load_dwordx4 v[116:119], v1, s[16:19], s60 offen offset:1024 sc1\n\t"
    "v_mfma_f32_16x16x32_bf16 v[120:123], a[48:51], a[96:99], 0\n\t"
    "v_cvt_pk_bf16_f32 v110, v80, v81\n\t"
    "s_waitcnt lgkmcnt(5)\n\t"
    "v_mfma_f32_16x16x32_bf16 v[124:127], a[136:139], a[72:75], 0\n\t"
    "ds_read_b128 a[116:119], v8 offset:26624\n\t"
    "v_cvt_pk_bf16_f32 v111, v82, v83\n\t"
    "v_mfma_f32_16x16x32_bf16 v[40:43], a[4:7], a[100:103], v[40:43]\n\t"
    "v_sub_f32 v102, v102, v240\n\t"
    "v_add_f32 v242, v100, v101\n\t"
    "v_mfma_f32_16x16x32_bf16 v[88:91], a[20:23], v[108:111], v[88:91]\n\t"
    "v_exp_f32 v102, v102\n\t"
    "v_mfma_f32_16x16x32_bf16 v[76:79], a[44:47], v[108:111], v[76:79]\n\t"
    "v_sub_f32 v103, v103, v240\n\t"
    "v_add_f32 v242, v242, v102\n\t"
    "v_mfma_f32_16x16x32_bf16 v[92:95], a[68:71], v[108:111], v[92:95]\n\t"
    "v_exp_f32 v103, v103\n\t"
    "v_mfma_f32_16x16x32_bf16 v[104:107], a[92:95], v[108:111], v[104:107]\n\t"
    "v_sub_f32 v72, v72, v240\n\t"
    "v_add_f32 v242, v242, v103\n\t"
    "v_mfma_f32_16x16x32_bf16 v[112:115], a[28:31], a[100:103], v[112:115]\n\t"
    "v_exp_f32 v72, v72\n\t"
    "v_mfma_f32_16x16x32_bf16 v[120:123], a[52:55], a[100:103], v[120:123]\n\t"
    "ds_read_b128 a[96:99], v8 offset:27648\n\t"
    "v_sub_f32 v73, v73, v240\n\t"
    "v_mfma_f32_16x16x32_bf16 v[124:127], a[140:143], a[76:79], v[124:127]\n\t"
    "v_exp_f32 v73, v73\n\t"
    "v_mfma_f32_16x16x32_bf16 v[40:43], a[8:11], a[124:127], v[40:43]\n\t"
    "v_add_f32 v242, v242, v72\n\t"
    "v_sub_f32 v74, v74, v240\n\t"
    "v_mfma_f32_16x16x32_bf16 v[112:115], a[32:35], a[124:127], v[112:115]\n\t"
    "ds_read_b128 a[100:103], v8 offset:28672\n\t"
    "ds_read_b128 a[120:123], v8 offset:29696\n\t"
    "v_mfma_f32_16x16x32_bf16 v[120:123], a[56:59], a[124:127], v[120:123]\n\t"
    "v_exp_f32 v74, v74\n\t"
    "v_mfma_f32_16x16x32_bf16 v[124:127], a[104:107], a[80:83], v[124:127]\n\t"
    "v_add_f32 v242, v242, v73\n\t"
    "v_sub_f32 v75, v75, v240\n\t"
    "v_mfma_f32_16x16x32_bf16 v[40:43], a[12:15], a[128:131], v[40:43]\n\t"
    "v_exp_f32 v75, v75\n\t"
    "v_mfma_f32_16x16x32_bf16 v[112:115], a[36:39], a[128:131], v[112:115]\n\t"
    "v_add_f32 v242, v242, v74\n\t"
    "v_sub_f32 v148, v148, v240\n\t"
    "v_mfma_f32_16x16x32_bf16 v[120:123], a[60:63], a[128:131], v[120:123]\n\t"
    "v_add_f32 v242, v242, v75\n\t"
    "s_waitcnt lgkmcnt(3)\n\t"
    "v_mfma_f32_16x16x32_bf16 v[124:127], a[108:111], a[84:87], v[124:127]\n\t"
    "ds_read_b128 a[104:107], v8 offset:30720\n\t"
    "ds_read_b128 a[124:127], v8 offset:31744\n\t"
    "v_mfma_f32_16x16x32_bf16 v[40:43], a[16:19], a[112:115], v[40:43]\n\t"
    "v_exp_f32 v148, v148\n\t"
    "v_mfma_f32_16x16x32_bf16 v[112:115], a[40:43], a[112:115], v[112:115]\n\t"
    "v_sub_f32 v149, v149, v240\n\t"
    "v_add_f32 v242, v242, v148\n\t"
    "v_mfma_f32_16x16x32_bf16 v[120:123], a[64:67], a[112:115], v[120:123]\n\t"
    "v_exp_f32 v149, v149\n\t"
    "v_mfma_f32_16x16x32_bf16 v[124:127], a[132:135], a[88:91], v[124:127]\n\t"
    "v_sub_f32 v150, v150, v240\n\t"
    "v_add_f32 v242, v242, v149\n\t"
    "v_mfma_f32_16x16x32_bf16 v[40:43], a[20:23], a[148:151], v[40:43]\n\t"
    "v_exp_f32 v150, v150\n\t"
    "v_mfma_f32_16x16x32_bf16 v[112:115], a[44:47], a[148:151], v[112:115]\n\t"
    "ds_read_b128 a[108:111], v8 offset:45056\n\t"
    "v_sub_f32 v151, v151, v240\n\t"
    "v_mfma_f32_16x16x32_bf16 v[120:123], a[68:71], a[148:151], v[120:123]\n\t"
    "v_exp_f32 v151, v151\n\t"
    "v_mfma_f32_16x16x32_bf16 v[124:127], a[144:147], a[92:95], v[124:127]\n\t"
    "v_cvt_pk_bf16_f32 v80, v40, v41\n\t"
    "s_waitcnt vmcnt(2)\n\t"
    "v_mfma_f32_16x16x32_bf16 v[108:111], a[0:3], a[116:119], 0\n\t"
    "ds_read_b128 a[112:115], v13 offset:6144\n\t"
    "v_cvt_pk_bf16_f32 v81, v42, v43\n\t"
    "v_mfma_f32_16x16x32_bf16 v[128:131], a[24:27], a[116:119], 0\n\t"
    "ds_read_b128 a[128:131], v13 offset:7168\n\t"
    "v_cvt_pk_bf16_f32 v82, v112, v113\n\t"
    "v_mfma_f32_16x16x32_bf16 v[132:135], a[48:51], a[116:119], 0\n\t"
    "ds_read_b128 a[132:135], v13 offset:8192\n\t"
    "s_waitcnt lgkmcnt(6)\n\t"
    "v_mfma_f32_16x16x32_bf16 v[108:111], a[4:7], a[96:99], v[108:111]\n\t"
    "ds_read_b128 a[136:139], v13 offset:9216\n\t"
    "v_cvt_pk_bf16_f32 v40, v124, v124\n\t"
    "v_mfma_f32_16x16x32_bf16 v[128:131], a[28:31], a[96:99], v[128:131]\n\t"
    "ds_read_b128 a[116:119], v8 offset:46080\n\t"
    "v_cvt_pk_bf16_f32 v41, v125, v125\n\t"
    "v_mfma_f32_16x16x32_bf16 v[132:135], a[52:55], a[96:99], v[132:135]\n\t"
    "ds_read_b128 a[140:143], v13 offset:10240\n\t"
    "v_cvt_pk_bf16_f32 v42, v126, v126\n\t"
    "v_mfma_f32_16x16x32_bf16 v[108:111], a[8:11], a[100:103], v[108:111]\n\t"
    "v_cvt_pk_bf16_f32 v43, v127, v127\n\t"
    "v_cvt_pk_bf16_f32 v83, v114, v115\n\t"
    "v_mfma_f32_16x16x32_bf16 v[128:131], a[32:35], a[100:103], v[128:131]\n\t"
    "ds_read_b128 a[96:99], v13 offset:11264\n\t"
    "v_cvt_pk_bf16_f32 v136, v120, v121\n\t"
    "v_mfma_f32_16x16x32_bf16 v[132:135], a[56:59], a[100:103], v[132:135]\n\t"
    "ds_write_b128 v30, v[40:43] offset:0\n\t"
    "v_mfma_f32_16x16x32_bf16 v[108:111], a[12:15], a[120:123], v[108:111]\n\t"
    "ds_read_b128 a[144:147], v13 offset:12288\n\t"
    "v_cvt_pk_bf16_f32 v137, v122, v123\n\t"
    "v_mfma_f32_16x16x32_bf16 v[128:131], a[36:39], a[120:123], v[128:131]\n\t"
    "ds_read_b128 a[100:103], v13 offset:13312\n\t"
    "v_add_f32 v242, v242, v150\n\t"
    "v_mfma_f32_16x16x32_bf16 v[132:135], a[60:63], a[120:123], v[132:135]\n\t"
    "ds_read_b128 a[148:151], v8 offset:47104\n\t"
    "s_waitcnt lgkmcnt(11)\n\t"
    "v_mfma_f32_16x16x32_bf16 v[108:111], a[16:19], a[104:107], v[108:111]\n\t"
    "ds_read_b128 a[152:155], v13 offset:14336\n\t"
    "v_sub_f32 v239, v239, v240\n\t"
    "v_mfma_f32_16x16x32_bf16 v[128:131], a[40:43], a[104:107], v[128:131]\n\t"
    "v_exp_f32 v239, v239\n\t"
    "v_mfma_f32_16x16x32_bf16 v[132:135], a[64:67], a[104:107], v[132:135]\n\t"
    "ds_read_b128 a[120:123], v13 offset:15360\n\t"
    "v_add_f32 v242, v242, v151\n\t"
    "v_mfma_f32_16x16x32_bf16 v[108:111], a[20:23], a[124:127], v[108:111]\n\t"
    "v_add_f32 v242, v242, v239\n\t"
    "v_cndmask_b32_e64 v240, v35, v104, s[22:23]\n\t"
    "v_mfma_f32_16x16x32_bf16 v[128:131], a[44:47], a[124:127], v[128:131]\n\t"
    "ds_read_b128 a[104:107], v13 offset:16384\n\t"
    "ds_read_b128 a[156:159], v8 offset:48128\n\t"
    "v_mfma_f32_16x16x32_bf16 v[132:135], a[68:71], a[124:127], v[132:135]\n\t"
    "ds_read_b128 a[160:163], v13 offset:17408\n\t"
    "v_max3_f32 v237, v88, v89, v90\n\t"
    "v_mfma_f32_16x16x32_bf16 v[40:43], a[0:3], a[108:111], 0\n\t"
    "v_cvt_pk_bf16_f32 v104, v108, v109\n\t"
    "s_waitcnt lgkmcnt(10)\n\t"
    "v_mfma_f32_16x16x32_bf16 v[112:115], a[112:115], a[72:75], 0\n\t"
    "v_cvt_pk_bf16_f32 v105, v110, v111\n\t"
    "v_cvt_pk_bf16_f32 v106, v128, v129\n\t"
    "v_mfma_f32_16x16x32_bf16 v[120:123], a[24:27], a[108:111], 0\n\t"
    "v_cvt_pk_bf16_f32 v107, v130, v131\n\t"
    "v_cvt_pk_bf16_f32 v124, v132, v133\n\t"
    "v_mfma_f32_16x16x32_bf16 v[112:115], a[128:131], a[76:79], v[112:115]\n\t"
    "ds_read_b128 a[112:115], v8 offset:49152\n\t"
    "ds_read_b128 a[124:127], v8 offset:50176\n\t"
    "v_mfma_f32_16x16x32_bf16 v[108:111], a[48:51], a[108:111], 0\n\t"
    "v_cvt_pk_bf16_f32 v125, v134, v135\n\t"
    "v_max3_f32 v237, v237, v91, v76\n\t"
    "v_mfma_f32_16x16x32_bf16 v[112:115], a[132:135], a[80:83], v[112:115]\n\t"
    "v_permlane32_swap_b32 v238, v242\n\t"
    "v_mfma_f32_16x16x32_bf16 v[112:115], a[136:139], a[84:87], v[112:115]\n\t"
    "v_add_f32 v238, v238, v242\n\t"
    "v_mov_b32 v242, v238\n\t"
    "v_mfma_f32_16x16x32_bf16 v[40:43], a[4:7], a[116:119], v[40:43]\n\t"
    "ds_read_b128 a[108:111], v8 offset:63488\n\t"
    "v_max3_f32 v237, v237, v77, v78\n\t"
    "v_mfma_f32_16x16x32_bf16 v[112:115], a[140:143], a[88:91], v[112:115]\n\t"
    "v_permlane16_swap_b32 v238, v242\n\t"
    "v_mfma_f32_16x16x32_bf16 v[120:123], a[28:31], a[116:119], v[120:123]\n\t"
    "v_add_f32 v238, v238, v242\n\t"
    "s_waitcnt lgkmcnt(6)\n\t"
    "v_mfma_f32_16x16x32_bf16 v[112:115], a[96:99], a[92:95], v[112:115]\n\t"
    "v_rcp_f32 v238, v238\n\t"
    "v_mfma_f32_16x16x32_bf16 v[108:111], a[52:55], a[116:119], v[108:111]\n\t"
    "v_mov_b32 v242, v238\n\t"
    "v_max3_f32 v237, v237, v79, v92\n\t"
    "v_mfma_f32_16x16x32_bf16 v[128:131], a[144:147], a[72:75], 0\n\t"
    "ds_read_b128 a[96:99], v8 offset:64512\n\t"
    "ds_read_b128 a[116:119], v9 offset:0\n\t"
    "v_mfma_f32_16x16x32_bf16 v[128:131], a[100:103], a[76:79], v[128:131]\n\t"
    "v_cvt_pk_bf16_f32 v132, v112, v112\n\t"
    "v_cvt_pk_bf16_f32 v133, v113, v113\n\t"
    "v_mfma_f32_16x16x32_bf16 v[40:43], a[8:11], a[148:151], v[40:43]\n\t"
    "v_cvt_pk_bf16_f32 v134, v114, v114\n\t"
    "v_cvt_pk_bf16_f32 v135, v115, v115\n\t"
    "v_mfma_f32_16x16x32_bf16 v[128:131], a[152:155], a[80:83], v[128:131]\n\t"
    "ds_write_b128 v30, v[132:135] offset:256\n\t"
    "v_mfma_f32_16x16x32_bf16 v[120:123], a[32:35], a[148:151], v[120:123]\n\t"
    "v_permlane32_swap_b32 v238, v242\n\t"
    "v_mfma_f32_16x16x32_bf16 v[128:131], a[120:123], a[84:87], v[128:131]\n\t"
    "v_mul_f32 v44, v44, v238\n\t"
    "v_mul_f32 v45, v45, v238\n\t"
    "v_mfma_f32_16x16x32_bf16 v[108:111], a[56:59], a[148:151], v[108:111]\n\t"
    "ds_read_b128 a[100:103], v9 offset:1024\n\t"
    "s_waitcnt lgkmcnt(5)\n\t"
    "v_mfma_f32_16x16x32_bf16 v[128:131], a[104:107], a[88:91], v[128:131]\n\t"
    "v_cvt_pk_bf16_f32 v112, v44, v45\n\t"
    "v_mul_f32 v46, v46, v238\n\t"
    "v_mfma_f32_16x16x32_bf16 v[128:131], a[160:163], a[92:95], v[128:131]\n\t"
    "v_mul_f32 v47, v47, v238\n\t"
    "v_cvt_pk_bf16_f32 v113, v46, v47\n\t"
    "v_mfma_f32_16x16x32_bf16 v[40:43], a[12:15], a[156:159], v[40:43]\n\t"
    "ds_read_b128 a[104:107], v9 offset:2048\n\t"
    "v_mul_f32 v68, v68, v238\n\t"
    "v_mfma_f32_16x16x32_bf16 v[120:123], a[36:39], a[156:159], v[120:123]\n\t"
    "v_mul_f32 v69, v69, v238\n\t"
    "v_cvt_pk_bf16_f32 v114, v68, v69\n\t"
    "v_mfma_f32_16x16x32_bf16 v[108:111], a[60:63], a[156:159], v[108:111]\n\t"
    "v_cvt_pk_bf16_f32 v44, v128, v128\n\t"
    "v_cvt_pk_bf16_f32 v45, v129, v129\n\t"
    "v_mfma_f32_16x16x32_bf16 v[40:43], a[16:19], a[112:115], v[40:43]\n\t"
    "ds_read_b128 a[120:123], v9 offset:3072\n\t"
    "v_cvt_pk_bf16_f32 v46, v130, v130\n\t"
    "v_mfma_f32_16x16x32_bf16 v[120:123], a[40:43], a[112:115], v[120:123]\n\t"
    "v_cvt_pk_bf16_f32 v47, v131, v131\n\t"
    "v_mul_f32 v70, v70, v238\n\t"
    "v_mfma_f32_16x16x32_bf16 v[108:111], a[64:67], a[112:115], v[108:111]\n\t"
    "ds_write_b128 v30, v[44:47] offset:512\n\t"
    "v_mfma_f32_16x16x32_bf16 v[40:43], a[20:23], a[124:127], v[40:43]\n\t"
    "v_mul_f32 v71, v71, v238\n\t"
    "v_cvt_pk_bf16_f32 v115, v70, v71\n\t"
    "v_mfma_f32_16x16x32_bf16 v[120:123], a[44:47], a[124:127], v[120:123]\n\t"
    "v_mul_f32 v84, v84, v238\n\t"
    "v_mul_f32 v85, v85, v238\n\t"
    "v_mfma_f32_16x16x32_bf16 v[44:47], v[112:115], v[80:83], 0\n\t"
    "v_cvt_pk_bf16_f32 v52, v84, v85\n\t"
    "v_mul_f32 v86, v86, v238\n\t"
    "v_mfma_f32_16x16x32_bf16 v[68:71], v[112:115], v[104:107], 0\n\t"
    "v_cvt_pk_bf16_f32 v128, v40, v41\n\t"
    "v_cvt_pk_bf16_f32 v129, v42, v43\n\t"
    "v_mfma_f32_16x16x32_bf16 v[108:111], a[68:71], a[124:127], v[108:111]\n\t"
    "v_cvt_pk_bf16_f32 v130, v120, v121\n\t"
    "s_waitcnt lgkmcnt(3)\n\t"
    "v_mfma_f32_16x16x32_bf16 v[132:135], a[0:3], a[108:111], 0\n\t"
    "v_cvt_pk_bf16_f32 v131, v122, v123\n\t"
    "v_mul_f32 v87, v87, v238\n\t"
    "v_mfma_f32_16x16x32_bf16 v[40:43], a[24:27], a[108:111], 0\n\t"
    "v_cvt_pk_bf16_f32 v53, v86, v87\n\t"
    "v_mul_f32 v241, v241, v238\n\t"
    "v_mfma_f32_16x16x32_bf16 v[120:123], v[112:115], v[128:131], 0\n\t"
    "v_cvt_pk_bf16_f32 v140, v108, v109\n\t"
    "v_cvt_pk_bf16_f32 v141, v110, v111\n\t"
    "v_mfma_f32_16x16x32_bf16 v[84:87], a[48:51], a[108:111], 0\n\t"
    "s_waitcnt vmcnt(0) lgkmcnt(0)\n\t"
    "s_cmp_eq_u32 s42, 0\n\t"
    "s_cbranch_scc1 Lchk%=\n\t"
    "Laback%=:\n\t"
    "; keep v[96:99] v[116:119]\n\t"
    "s_barrier\n\t"
    "v_mfma_f32_16x16x32_bf16 v[132:135], a[4:7], a[96:99], v[132:135]\n\t"
    "ds_read2_b32 v[138:139], v31 offset0:0 offset1:0\n\t"
    "ds_read2_b32 v[126:127], v31 offset0:16 offset1:16\n\t"
    "v_mfma_f32_16x16x32_bf16 v[40:43], a[28:31], a[96:99], v[40:43]\n\t"
    "ds_read2_b32 v[142:143], v31 offset0:32 offset1:32\n\t"
    "v_cvt_pk_bf16_f32 v54, v241, 0\n\t"
    "v_mfma_f32_16x16x32_bf16 v[84:87], a[52:55], a[96:99], v[84:87]\n\t"
    "ds_read_b128 a[108:111], v8 offset:14336\n\t"
    "v_mul_f32 v100, v100, v242\n\t"
    "v_mfma_f32_16x16x32_bf16 v[132:135], a[8:11], a[116:119], v[132:135]\n\t"
    "ds_read_b128 a[112:115], v8 offset:15360\n\t"
    "v_mul_f32 v101, v101, v242\n\t"
    "v_mfma_f32_16x16x32_bf16 v[40:43], a[32:35], a[116:119], v[40:43]\n\t"
    "ds_read_b128 a[96:99], v8 offset:16384\n\t"
    "v_cvt_pk_bf16_f32 v96, v100, v101\n\t"
    "v_mfma_f32_16x16x32_bf16 v[84:87], a[56:59], a[116:119], v[84:87]\n\t"
    "ds_read_b128 a[124:127], v8 offset:17408\n\t"
    "v_mul_f32 v102, v102, v242\n\t"
    "v_mfma_f32_16x16x32_bf16 v[132:135], a[12:15], a[100:103], v[132:135]\n\t"
    "ds_read_b128 a[128:131], v8 offset:18432\n\t"
    "v_mul_f32 v103, v103, v242\n\t"
    "v_mfma_f32_16x16x32_bf16 v[40:43], a[36:39], a[100:103], v[40:43]\n\t"
    "v_cvt_pk_bf16_f32 v97, v102, v103\n\t"
    "v_mul_f32 v72, v72, v242\n\t"
    "v_mfma_f32_16x16x32_bf16 v[84:87], a[60:63], a[100:103], v[84:87]\n\t"
    "ds_read2_b32 v[110:111], v31 offset0:48 offset1:48\n\t"
    "v_mul_f32 v73, v73, v242\n\t"
    "v_mfma_f32_16x16x32_bf16 v[132:135], a[16:19], a[104:107], v[132:135]\n\t"
    "ds_read_b128 a[116:119], v8 offset:19456\n\t"
    "v_cvt_pk_bf16_f32 v98, v72, v73\n\t"
    "s_waitcnt lgkmcnt(9)\n\t"
    "v_mfma_f32_16x16x32_bf16 v[44:47], v[52:55], v[136:139], v[44:47]\n\t"
    "v_mul_f32 v74, v74, v242\n\t"
    "v_mul_f32 v75, v75, v242\n\t"
    "s_waitcnt lgkmcnt(8)\n\t"
    "v_mfma_f32_16x16x32_bf16 v[68:71], v[52:55], v[124:127], v[68:71]\n\t"
    "v_cvt_pk_bf16_f32 v99, v74, v75\n\t"
    "v_mul_f32 v148, v148, v242\n\t"
    "s_waitcnt lgkmcnt(7)\n\t"
    "v_mfma_f32_16x16x32_bf16 v[120:123], v[52:55], v[140:143], v[120:123]\n\t"
    "v_mul_f32 v149, v149, v242\n\t"
    "v_cvt_pk_bf16_f32 v56, v148, v149\n\t"
    "v_mfma_f32_16x16x32_bf16 v[72:75], v[96:99], v[80:83], 0\n\t"
    "v_mul_f32 v150, v150, v242\n\t"
    "v_cvt_pk_bf16_f32 v234, v44, v68\n\t"
    "v_mfma_f32_16x16x32_bf16 v[100:103], v[96:99], v[104:107], 0\n\t"
    "v_cvt_pk_bf16_f32 v232, v45, v69\n\t"
    "v_cvt_pk_bf16_f32 v230, v46, v70\n\t"
    "v_mfma_f32_16x16x32_bf16 v[116:119], v[96:99], v[128:131], 0\n\t"
    "v_cvt_pk_bf16_f32 v228, v47, v71\n\t"
    "v_mul_f32 v151, v151, v242\n\t"
    "v_mfma_f32_16x16x32_bf16 v[40:43], a[40:43], a[104:107], v[40:43]\n\t"
    "v_cvt_pk_bf16_f32 v57, v150, v151\n\t"
    "v_mul_f32 v239, v239, v242\n\t"
    "v_mfma_f32_16x16x32_bf16 v[84:87], a[64:67], a[104:107], v[84:87]\n\t"
    "v_cvt_pk_bf16_f32 v58, v239, 0\n\t"
    "v_max3_f32 v237, v237, v93, v94\n\t"
    "Lst2%=:\n\t"
    "s_cbranch_execz Lst3%=\n\t"
    "v_mfma_f32_16x16x32_bf16 v[132:135], a[20:23], a[120:123], v[132:135]\n\t"
    "ds_read_b128 a[100:103], v8 offset:32768\n\t"
    "v_max3_f32 v237, v237, v95, v240\n\t"
    "v_mfma_f32_16x16x32_bf16 v[72:75], v[56:59], v[136:139], v[72:75]\n\t"
    "ds_read_b128 a[104:107], v8 offset:33792\n\t"
    "v_mov_b32 v242, v237\n\t"
    "v_mfma_f32_16x16x32_bf16 v[100:103], v[56:59], v[124:127], v[100:103]\n\t"
    "ds_read_b128 a[132:135], v8 offset:34816\n\t"
    "s_waitcnt lgkmcnt(4)\n\t"
    "v_mfma_f32_16x16x32_bf16 v[116:119], v[56:59], v[140:143], v[116:119]\n\t"
    "v_cvt_pk_bf16_f32 v44, v132, v133\n\t"
    "v_cvt_pk_bf16_f32 v45, v134, v135\n\t"
    "v_mfma_f32_16x16x32_bf16 v[40:43], a[44:47], a[120:123], v[40:43]\n\t"
    "v_permlane32_swap_b32 v237, v242\n\t"
    "v_mfma_f32_16x16x32_bf16 v[84:87], a[68:71], a[120:123], v[84:87]\n\t"
    "v_cvt_pk_bf16_f32 v238, v72, v100\n\t"
    "v_cvt_pk_bf16_f32 v226, v73, v101\n\t"
    "v_mfma_f32_16x16x32_bf16 v[68:71], a[0:3], a[108:111], 0\n\t"
    "v_cvt_pk_bf16_f32 v224, v74, v102\n\t"
    "v_cvt_pk_bf16_f32 v222, v75, v103\n\t"
    "v_mfma_f32_16x16x32_bf16 v[132:135], a[24:27], a[108:111], 0\n\t"
    "v_cvt_pk_bf16_f32 v46, v40, v41\n\t"
    "v_cvt_pk_bf16_f32 v47, v42, v43\n\t"
    "v_mfma_f32_16x16x32_bf16 v[144:147], a[48:51], a[108:111], 0\n\t"
    "v_cvt_pk_bf16_f32 v108, v84, v85\n\t"
    "v_cvt_pk_bf16_f32 v109, v86, v87\n\t"
    "v_mfma_f32_16x16x32_bf16 v[72:75], v[112:115], v[44:47], 0\n\t"
    "ds_read2_b32 v[42:43], v31 offset0:64 offset1:64\n\t"
    "v_max_f32 v237, v237, v242\n\t"
    "v_mfma_f32_16x16x32_bf16 v[72:75], v[52:55], v[108:111], v[72:75]\n\t"
    "ds_read_b128 a[108:111], v8 offset:35840\n\t"
    "v_mov_b32 v242, v237\n\t"
    "v_mfma_f32_16x16x32_bf16 v[84:87], v[96:99], v[44:47], 0\n\t"
    "ds_read_b128 a[120:123], v8 offset:36864\n\t"
    "v_mfma_f32_16x16x32_bf16 v[84:87], v[56:59], v[108:111], v[84:87]\n\t"
    "v_permlane16_swap_b32 v237, v242\n\t"
    "v_mfma_f32_16x16x32_bf16 v[68:71], a[4:7], a[112:115], v[68:71]\n\t"
    "v_cvt_pk_bf16_f32 v235, v120, v72\n\t"
    "v_cvt_pk_bf16_f32 v233, v121, v73\n\t"
    "v_mfma_f32_16x16x32_bf16 v[132:135], a[28:31], a[112:115], v[132:135]\n\t"
    "buffer_store_dwordx2 v[234:235], v16, s[12:15], s49 offen offset:0 sc1\n\t"
    "v_mfma_f32_16x16x32_bf16 v[144:147], a[52:55], a[112:115], v[144:147]\n\t"
    "v_cvt_pk_bf16_f32 v231, v122, v74\n\t"
    "v_cvt_pk_bf16_f32 v229, v123, v75\n\t"
    "v_mfma_f32_16x16x32_bf16 v[68:71], a[8:11], a[96:99], v[68:71]\n\t"
    "v_cvt_pk_bf16_f32 v239, v116, v84\n\t"
    "v_cvt_pk_bf16_f32 v227, v117, v85\n\t"
    "v_mfma_f32_16x16x32_bf16 v[132:135], a[32:35], a[96:99], v[132:135]\n\t"
    "v_cvt_pk_bf16_f32 v225, v118, v86\n\t"
    "v_cvt_pk_bf16_f32 v223, v119, v87\n\t"
    "v_mfma_f32_16x16x32_bf16 v[144:147], a[56:59], a[96:99], v[144:147]\n\t"
    "buffer_store_dwordx2 v[232:233], v17, s[12:15], s49 offen offset:384 sc1\n\t"
    "v_mfma_f32_16x16x32_bf16 v[68:71], a[12:15], a[124:127], v[68:71]\n\t"
    "ds_read_b128 a[112:115], v8 offset:37888\n\t"
    "v_max_f32 v237, v237, v242\n\t"
    "v_mfma_f32_16x16x32_bf16 v[132:135], a[36:39], a[124:127], v[132:135]\n\t"
    "v_sub_f32 v88, v88, v237\n\t"
    "v_sub_f32 v89, v89, v237\n\t"
    "v_mfma_f32_16x16x32_bf16 v[144:147], a[60:63], a[124:127], v[144:147]\n\t"
    "v_exp_f32 v88, v88\n\t"
    "v_mfma_f32_16x16x32_bf16 v[68:71], a[16:19], a[128:131], v[68:71]\n\t"
    "buffer_store_dwordx2 v[230:231], v18, s[12:15], s49 offen offset:768 sc1\n\t"
    "v_mfma_f32_16x16x32_bf16 v[132:135], a[40:43], a[128:131], v[132:135]\n\t"
    "v_exp_f32 v89, v89\n\t"
    "v_mfma_f32_16x16x32_bf16 v[144:147], a[64:67], a[128:131], v[144:147]\n\t"
    "v_sub_f32 v90, v90, v237\n\t"
    "s_waitcnt lgkmcnt(1)\n\t"
    "v_mfma_f32_16x16x32_bf16 v[68:71], a[20:23], a[116:119], v[68:71]\n\t"
    "ds_read_b128 a[96:99], v8 offset:51200\n\t"
    "ds_read_b128 a[124:127], v8 offset:52224\n\t"
    "v_mfma_f32_16x16x32_bf16 v[132:135], a[44:47], a[116:119], v[132:135]\n\t"
    "buffer_store_dwordx2 v[228:229], v19, s[12:15], s49 offen offset:1152 sc1\n\t"
    "v_mfma_f32_16x16x32_bf16 v[144:147], a[68:71], a[116:119], v[144:147]\n\t"
    "v_exp_f32 v90, v90\n\t"
    "v_mfma_f32_16x16x32_bf16 v[72:75], a[0:3], a[100:103], 0\n\t"
    "v_cvt_pk_bf16_f32 v84, v68, v69\n\t"
    "v_cvt_pk_bf16_f32 v85, v70, v71\n\t"
    "v_mfma_f32_16x16x32_bf16 v[100:103], a[24:27], a[100:103], 0\n\t"
    "v_cvt_pk_bf16_f32 v86, v132, v133\n\t"
    "v_cvt_pk_bf16_f32 v87, v134, v135\n\t"
    "v_mfma_f32_16x16x32_bf16 v[116:119], a[48:51], a[100:103], 0\n\t"
    "buffer_store_dwordx2 v[238:239], v20, s[12:15], s50 offen offset:0 sc1\n\t"
    "v_mfma_f32_16x16x32_bf16 v[68:71], v[112:115], v[84:87], 0\n\t"
    "v_cvt_pk_bf16_f32 v40, v144, v145\n\t"
    "v_cvt_pk_bf16_f32 v41, v146, v147\n\t"
    "v_mfma_f32_16x16x32_bf16 v[120:123], v[96:99], v[84:87], 0\n\t"
    "v_add_f32 v242, v88, v89\n\t"
    "v_sub_f32 v91, v91, v237\n\t"
    "v_mfma_f32_16x16x32_bf16 v[68:71], v[52:55], v[40:43], v[68:71]\n\t"
    "v_exp_f32 v91, v91\n\t"
    "v_mfma_f32_16x16x32_bf16 v[120:123], v[56:59], v[40:43], v[120:123]\n\t"
    "buffer_store_dwordx2 v[226:227], v21, s[12:15], s50 offen offset:384 sc1\n\t"
    "v_mfma_f32_16x16x32_bf16 v[72:75], a[4:7], a[104:107], v[72:75]\n\t"
    "ds_read_b128 a[100:103], v8 offset:53248\n\t"
    "v_add_f32 v242, v242, v90\n\t"
    "v_mfma_f32_16x16x32_bf16 v[100:103], a[28:31], a[104:107], v[100:103]\n\t"
    "v_sub_f32 v76, v76, v237\n\t"
    "v_add_f32 v242, v242, v91\n\t"
    "v_mfma_f32_16x16x32_bf16 v[116:119], a[52:55], a[104:107], v[116:119]\n\t"
    "v_exp_f32 v76, v76\n\t"
    "v_mfma_f32_16x16x32_bf16 v[72:75], a[8:11], a[132:135], v[72:75]\n\t"
    "ds_read_b128 a[116:119], v8 offset:54272\n\t"
    "v_sub_f32 v77, v77, v237\n\t"
    "v_mfma_f32_16x16x32_bf16 v[100:103], a[32:35], a[132:135], v[100:103]\n\t"
    "buffer_store_dwordx2 v[224:225], v22, s[12:15], s50 offen offset:768 sc1\n\t"
    "v_mfma_f32_16x16x32_bf16 v[116:119], a[56:59], a[132:135], v[116:119]\n\t"
    "v_exp_f32 v77, v77\n\t"
    "v_mfma_f32_16x16x32_bf16 v[72:75], a[12:15], a[108:111], v[72:75]\n\t"
    "ds_read_b128 a[104:107], v8 offset:55296\n\t"
    "v_add_f32 v242, v242, v76\n\t"
    "v_mfma_f32_16x16x32_bf16 v[100:103], a[36:39], a[108:111], v[100:103]\n\t"
    "v_sub_f32 v78, v78, v237\n\t"
    "v_add_f32 v242, v242, v77\n\t"
    "v_mfma_f32_16x16x32_bf16 v[116:119], a[60:63], a[108:111], v[116:119]\n\t"
    "ds_read2_b32 v[134:135], v31 offset0:80 offset1:80\n\t"
    "ds_read_b128 a[128:131], v8 offset:56320\n\t"
    "v_mfma_f32_16x16x32_bf16 v[72:75], a[16:19], a[120:123], v[72:75]\n\t"
    "buffer_store_dwordx2 v[222:223], v23, s[12:15], s50 offen offset:1152 sc1\n\t"
    "v_mfma_f32_16x16x32_bf16 v[100:103], a[40:43], a[120:123], v[100:103]\n\t"
    "v_exp_f32 v78, v78\n\t"
    "v_mfma_f32_16x16x32_bf16 v[116:119], a[64:67], a[120:123], v[116:119]\n\t"
    "v_sub_f32 v79, v79, v237\n\t"
    "s_waitcnt lgkmcnt(5)\n\t"
    "v_mfma_f32_16x16x32_bf16 v[72:75], a[20:23], a[112:115], v[72:75]\n\t"
    "ds_read_b128 a[108:111], v9 offset:4096\n\t"
    "ds_read_b128 a[120:123], v9 offset:5120\n\t"
    "v_mfma_f32_16x16x32_bf16 v[100:103], a[44:47], a[112:115], v[100:103]\n\t"
    "v_exp_f32 v79, v79\n\t"
    "v_mfma_f32_16x16x32_bf16 v[116:119], a[68:71], a[112:115], v[116:119]\n\t"
    "v_add_f32 v242, v242, v78\n\t"
    "v_sub_f32 v92, v92, v237\n\t"
    "v_mfma_f32_16x16x32_bf16 v[144:147], a[0:3], a[96:99], 0\n\t"
    "v_cvt_pk_bf16_f32 v148, v72, v73\n\t"
    "v_cvt_pk_bf16_f32 v149, v74, v75\n\t"
    "v_mfma_f32_16x16x32_bf16 v[152:155], a[24:27], a[96:99], 0\n\t"
    "v_cvt_pk_bf16_f32 v150, v100, v101\n\t"
    "v_cvt_pk_bf16_f32 v151, v102, v103\n\t"
    "v_mfma_f32_16x16x32_bf16 v[156:159], a[48:51], a[96:99], 0\n\t"
    "v_cvt_pk_bf16_f32 v132, v116, v117\n\t"
    "v_cvt_pk_bf16_f32 v133, v118, v119\n\t"
    "v_mfma_f32_16x16x32_bf16 v[72:75], v[112:115], v[148:151], 0\n\t"
    "v_exp_f32 v92, v92\n\t"
    "s_waitcnt lgkmcnt(3)\n\t"
    "v_mfma_f32_16x16x32_bf16 v[72:75], v[52:55], v[132:135], v[72:75]\n\t"
    "v_add_f32 v242, v242, v79\n\t"
    "v_sub_f32 v93, v93, v237\n\t"
    "v_mfma_f32_16x16x32_bf16 v[100:103], v[96:99], v[148:151], 0\n\t"
    "v_exp_f32 v93, v93\n\t"
    "v_mfma_f32_16x16x32_bf16 v[100:103], v[56:59], v[132:135], v[100:103]\n\t"
    "v_add_f32 v242, v242, v92\n\t"
    "v_sub_f32 v94, v94, v237\n\t"
    "v_mfma_f32_16x16x32_bf16 v[144:147], a[4:7], a[124:127], v[144:147]\n\t"
    "ds_read_b128 a[96:99], v9 offset:6144\n\t"
    "ds_read2_b32 v[118:119], v31 offset0:96 offset1:96\n\t"
    "v_mfma_f32_16x16x32_bf16 v[152:155], a[28:31], a[124:127], v[152:155]\n\t"
    "v_cvt_pk_bf16_f32 v241, v68, v72\n\t"
    "v_cvt_pk_bf16_f32 v239, v69, v73\n\t"
    "v_mfma_f32_16x16x32_bf16 v[156:159], a[52:55], a[124:127], v[156:159]\n\t"
    "buffer_store_dword v241, v243, s[12:15], s49 offen offset:128 sc1\n\t"
    "v_cvt_pk_bf16_f32 v238, v70, v74\n\t"
    "v_mfma_f32_16x16x32_bf16 v[144:147], a[8:11], a[100:103], v[144:147]\n\t"
    "ds_read_b128 a[112:115], v9 offset:7168\n\t"
    "v_cvt_pk_bf16_f32 v236, v71, v75\n\t"
    "v_mfma_f32_16x16x32_bf16 v[152:155], a[32:35], a[100:103], v[152:155]\n\t"
    "buffer_store_dword v239, v244, s[12:15], s49 offen offset:512 sc1\n\t"
    "v_cvt_pk_bf16_f32 v241, v120, v100\n\t"
    "v_mfma_f32_16x16x32_bf16 v[156:159], a[56:59], a[100:103], v[156:159]\n\t"
    "v_cvt_pk_bf16_f32 v235, v121, v101\n\t"
    "v_cvt_pk_bf16_f32 v234, v122, v102\n\t"
    "v_mfma_f32_16x16x32_bf16 v[144:147], a[12:15], a[116:119], v[144:147]\n\t"
    "ds_read_b128 a[124:127], v9 offset:8192\n\t"
    "ds_read_b128 a[100:103], v9 offset:9216\n\t"
    "v_mfma_f32_16x16x32_bf16 v[152:155], a[36:39], a[116:119], v[152:155]\n\t"
    "buffer_store_dword v238, v245, s[12:15], s49 offen offset:896 sc1\n\t"
    "v_cvt_pk_bf16_f32 v239, v123, v103\n\t"
    "v_mfma_f32_16x16x32_bf16 v[156:159], a[60:63], a[116:119], v[156:159]\n\t"
    "v_exp_f32 v94, v94\n\t"
    "v_mfma_f32_16x16x32_bf16 v[144:147], a[16:19], a[104:107], v[144:147]\n\t"
    "buffer_store_dword v236, v246, s[12:15], s49 offen offset:1280 sc1\n\t"
    "v_add_f32 v242, v242, v93\n\t"
    "v_mfma_f32_16x16x32_bf16 v[152:155], a[40:43], a[104:107], v[152:155]\n\t"
    "v_sub_f32 v95, v95, v237\n\t"
    "v_add_f32 v242, v242, v94\n\t"
    "v_mfma_f32_16x16x32_bf16 v[156:159], a[64:67], a[104:107], v[156:159]\n\t"
    "buffer_store_dword v241, v247, s[12:15], s50 offen offset:128 sc1\n\t"
    "s_waitcnt lgkmcnt(5)\n\t"
    "v_mfma_f32_16x16x32_bf16 v[144:147], a[20:23], a[128:131], v[144:147]\n\t"
    "ds_read_b128 a[116:119], v8 offset:20480\n\t"
    "ds_read_b128 a[104:107], v8 offset:21504\n\t"
    "v_mfma_f32_16x16x32_bf16 v[152:155], a[44:47], a[128:131], v[152:155]\n\t"
    "buffer_store_dword v235, v248, s[12:15], s50 offen offset:512 sc1\n\t"
    "v_sub_f32 v240, v240, v237\n\t"
    "v_mfma_f32_16x16x32_bf16 v[156:159], a[68:71], a[128:131], v[156:159]\n\t"
    "ds_read_b128 a[132:135], v33 offset:24576\n\t"
    "v_mfma_f32_16x16x32_bf16 v[68:71], a[0:3], a[108:111], 0\n\t"
    "buffer_store_dword v234, v249, s[12:15], s50 offen offset:896 sc1\n\t"
    "v_cvt_pk_bf16_f32 v72, v144, v145\n\t"
    "v_mfma_f32_16x16x32_bf16 v[100:103], a[24:27], a[108:111], 0\n\t"
    "ds_read_b128 a[128:131], v33 offset:25600\n\t"
    "ds_read2_b32 v[122:123], v31 offset0:112 offset1:112\n\t"
    "v_mfma_f32_16x16x32_bf16 v[160:163], a[48:51], a[108:111], 0\n\t"
    "buffer_store_dword v239, v250, s[12:15], s50 offen offset:1280 sc1\n\t"
    "v_cvt_pk_bf16_f32 v73, v146, v147\n\t"
    "v_mfma_f32_16x16x32_bf16 v[68:71], a[4:7], a[120:123], v[68:71]\n\t"
    "v_cvt_pk_bf16_f32 v74, v152, v153\n\t"
    "v_cvt_pk_bf16_f32 v75, v154, v155\n\t"
    "v_mfma_f32_16x16x32_bf16 v[100:103], a[28:31], a[120:123], v[100:103]\n\t"
    "ds_read_b128 a[108:111], v33 offset:26624\n\t"
    "v_cvt_pk_bf16_f32 v116, v156, v157\n\t"
    "v_mfma_f32_16x16x32_bf16 v[144:147], v[112:115], v[72:75], 0\n\t"
    "ds_read_b128 a[136:139], v8 offset:22528\n\t"
    "ds_read_b128 a[140:143], v8 offset:23552\n\t"
    "v_mfma_f32_16x16x32_bf16 v[152:155], v[96:99], v[72:75], 0\n\t"
    "ds_read_b128 a[144:147], v33 offset:27648\n\t"
    "v_cvt_pk_bf16_f32 v117, v158, v159\n\t"
    "v_mfma_f32_16x16x32_bf16 v[160:163], a[52:55], a[120:123], v[160:163]\n\t"
    "ds_read_b128 a[148:151], v33 offset:28672\n\t"
    "s_waitcnt lgkmcnt(10)\n\t"
    "v_mfma_f32_16x16x32_bf16 v[144:147], v[52:55], v[116:119], v[144:147]\n\t"
    "v_exp_f32 v95, v95\n\t"
    "v_mfma_f32_16x16x32_bf16 v[152:155], v[56:59], v[116:119], v[152:155]\n\t"
    "v_exp_f32 v240, v240\n\t"
    "v_mfma_f32_16x16x32_bf16 v[68:71], a[8:11], a[96:99], v[68:71]\n\t"
    "ds_read_b128 a[120:123], v33 offset:29696\n\t"
    "v_add_f32 v242, v242, v95\n\t"
    "v_mfma_f32_16x16x32_bf16 v[100:103], a[32:35], a[96:99], v[100:103]\n\t"
    "ds_read_b128 a[152:155], v8 offset:24576\n\t"
    "v_add_f32 v242, v242, v240\n\t"
    "v_mfma_f32_16x16x32_bf16 v[160:163], a[56:59], a[96:99], v[160:163]\n\t"
    "ds_read_b128 a[156:159], v33 offset:49152\n\t"
    "v_mov_b32 v241, v242\n\t"
    "v_mfma_f32_16x16x32_bf16 v[68:71], a[12:15], a[112:115], v[68:71]\n\t"
    "ds_read_b128 a[160:163], v33 offset:50176\n\t"
    "v_mfma_f32_16x16x32_bf16 v[100:103], a[36:39], a[112:115], v[100:103]\n\t"
    "v_permlane32_swap_b32 v242, v241\n\t"
    "v_mfma_f32_16x16x32_bf16 v[160:163], a[60:63], a[112:115], v[160:163]\n\t"
    "ds_read_b128 a[96:99], v33 offset:51200\n\t"
    "v_add_f32 v242, v242, v241\n\t"
    "v_mfma_f32_16x16x32_bf16 v[68:71], a[16:19], a[124:127], v[68:71]\n\t"
    "ds_read_b128 a[164:167], v8 offset:25600\n\t"
    "v_mov_b32 v241, v242\n\t"
    "v_mfma_f32_16x16x32_bf16 v[100:103], a[40:43], a[124:127], v[100:103]\n\t"
    "ds_read_b128 a[112:115], v33 offset:52224\n\t"
    "s_waitcnt lgkmcnt(11)\n\t"
    "v_mfma_f32_16x16x32_bf16 v[160:163], a[64:67], a[124:127], v[160:163]\n\t"
    "v_permlane16_swap_b32 v242, v241\n\t"
    "v_mfma_f32_16x16x32_bf16 v[68:71], a[20:23], a[100:103], v[68:71]\n\t"
    "ds_read_b128 a[168:171], v33 offset:53248\n\t"
    "v_add_f32 v242, v242, v241\n\t"
    "v_mfma_f32_16x16x32_bf16 v[100:103], a[44:47], a[100:103], v[100:103]\n\t"
    "v_rcp_f32 v242, v242\n\t"
    "v_mfma_f32_16x16x32_bf16 v[160:163], a[68:71], a[100:103], v[160:163]\n\t"
    "v_mul_f32 v88, v88, v242\n\t"
    "v_mul_f32 v89, v89, v242\n\t"
    "v_mfma_f32_16x16x32_bf16 v[156:159], a[0:3], a[116:119], 0\n\t"
    "v_cvt_pk_bf16_f32 v164, v68, v69\n\t"
    "v_cvt_pk_bf16_f32 v165, v70, v71\n\t"
    "v_mfma_f32_16x16x32_bf16 v[168:171], a[24:27], a[116:119], 0\n\t"
    "v_cvt_pk_bf16_f32 v166, v100, v101\n\t"
    "v_cvt_pk_bf16_f32 v167, v102, v103\n\t"
    "v_mfma_f32_16x16x32_bf16 v[172:175], a[132:135], a[72:75], 0\n\t"
    "v_cvt_pk_bf16_f32 v120, v160, v161\n\t"
    "v_cvt_pk_bf16_f32 v121, v162, v163\n\t"
    "v_mfma_f32_16x16x32_bf16 v[68:71], v[112:115], v[164:167], 0\n\t"
    "v_cvt_pk_bf16_f32 v100, v88, v89\n\t"
    "v_mul_f32 v90, v90, v242\n\t"
    "v_mfma_f32_16x16x32_bf16 v[68:71], v[52:55], v[120:123], v[68:71]\n\t"
    "v_mul_f32 v91, v91, v242\n\t"
    "v_cvt_pk_bf16_f32 v101, v90, v91\n\t"
    "v_mfma_f32_16x16x32_bf16 v[160:163], v[96:99], v[164:167], 0\n\t"
    "v_mul_f32 v76, v76, v242\n\t"
    "v_mul_f32 v77, v77, v242\n\t"
    "v_mfma_f32_16x16x32_bf16 v[160:163], v[56:59], v[120:123], v[160:163]\n\t"
    "v_cvt_pk_bf16_f32 v102, v76, v77\n\t"
    "v_mul_f32 v78, v78, v242\n\t"
    "v_mfma_f32_16x16x32_bf16 v[88:91], a[48:51], a[116:119], 0\n\t"
    "v_cvt_pk_bf16_f32 v241, v144, v68\n\t"
    "v_cvt_pk_bf16_f32 v239, v145, v69\n\t"
    "v_mfma_f32_16x16x32_bf16 v[172:175], a[128:131], a[76:79], v[172:175]\n\t"
    "buffer_store_dword v241, v243, s[12:15], s49 offen offset:192 sc1\n\t"
    "v_cvt_pk_bf16_f32 v238, v146, v70\n\t"
    "v_mfma_f32_16x16x32_bf16 v[156:159], a[4:7], a[104:107], v[156:159]\n\t"
    "v_cvt_pk_bf16_f32 v237, v147, v71\n\t"
    "v_cvt_pk_bf16_f32 v236, v152, v160\n\t"
    "v_mfma_f32_16x16x32_bf16 v[168:171], a[28:31], a[104:107], v[168:171]\n\t"
    "buffer_store_dword v239, v244, s[12:15], s49 offen offset:576 sc1\n\t"
    "v_cvt_pk_bf16_f32 v241, v153, v161\n\t"
    "v_mfma_f32_16x16x32_bf16 v[172:175], a[108:111], a[80:83], v[172:175]\n\t"
    "v_cvt_pk_bf16_f32 v235, v154, v162\n\t"
    "v_cvt_pk_bf16_f32 v234, v155, v163\n\t"
    "v_mfma_f32_16x16x32_bf16 v[88:91], a[52:55], a[104:107], v[88:91]\n\t"
    "ds_read_b128 a[100:103], v33 offset:54272\n\t"
    "s_waitcnt lgkmcnt(1)\n\t"
    "v_mfma_f32_16x16x32_bf16 v[172:175], a[144:147], a[84:87], v[172:175]\n\t"
    "ds_read_b128 a[108:111], v8 offset:38912\n\t"
    "v_mul_f32 v79, v79, v242\n\t"
    "v_mfma_f32_16x16x32_bf16 v[156:159], a[8:11], a[136:139], v[156:159]\n\t"
    "buffer_store_dword v238, v245, s[12:15], s49 offen offset:960 sc1\n\t"
    "v_cvt_pk_bf16_f32 v103, v78, v79\n\t"
    "v_mfma_f32_16x16x32_bf16 v[168:171], a[32:35], a[136:139], v[168:171]\n\t"
    "v_mul_f32 v92, v92, v242\n\t"
    "v_mul_f32 v93, v93, v242\n\t"
    "v_mfma_f32_16x16x32_bf16 v[68:71], v[100:103], v[80:83], 0\n\t"
    "buffer_store_dword v237, v246, s[12:15], s49 offen offset:1344 sc1\n\t"
    "v_cvt_pk_bf16_f32 v64, v92, v93\n\t"
    "v_mfma_f32_16x16x32_bf16 v[172:175], a[148:151], a[88:91], v[172:175]\n\t"
    "ds_read_b128 a[104:107], v8 offset:39936\n\t"
    "v_mul_f32 v94, v94, v242\n\t"
    "v_mfma_f32_16x16x32_bf16 v[76:79], v[100:103], v[104:107], 0\n\t"
    "buffer_store_dword v236, v247, s[12:15], s50 offen offset:192 sc1\n\t"
    "v_mul_f32 v95, v95, v242\n\t"
    "v_mfma_f32_16x16x32_bf16 v[144:147], v[100:103], v[128:131], 0\n\t"
    "v_cvt_pk_bf16_f32 v65, v94, v95\n\t"
    "v_mul_f32 v240, v240, v242\n\t"
    "v_mfma_f32_16x16x32_bf16 v[152:155], v[100:103], v[44:47], 0\n\t"
    "buffer_store_dword v241, v248, s[12:15], s50 offen offset:576 sc1\n\t"
    "v_cvt_pk_bf16_f32 v66, v240, 0\n\t"
    "v_mfma_f32_16x16x32_bf16 v[92:95], v[100:103], v[84:87], 0\n\t"
    "ds_read2_b32 v[162:163], v31 offset0:128 offset1:128\n\t"
    "ds_read_b128 a[116:119], v8 offset:40960\n\t"
    "v_mfma_f32_16x16x32_bf16 v[68:71], v[64:67], v[136:139], v[68:71]\n\t"
    "buffer_store_dword v235, v249, s[12:15], s50 offen offset:960 sc1\n\t"
    "v_mfma_f32_16x16x32_bf16 v[76:79], v[64:67], v[124:127], v[76:79]\n\t"
    "ds_read_b128 a[124:127], v8 offset:41984\n\t"
    "v_mfma_f32_16x16x32_bf16 v[144:147], v[64:67], v[140:143], v[144:147]\n\t"
    "buffer_store_dword v234, v250, s[12:15], s50 offen offset:1344 sc1\n\t"
    "v_mfma_f32_16x16x32_bf16 v[152:155], v[64:67], v[108:111], v[152:155]\n\t"
    "ds_read_b128 a[128:131], v8 offset:43008\n\t"
    "v_mfma_f32_16x16x32_bf16 v[92:95], v[64:67], v[40:43], v[92:95]\n\t"
    "v_cvt_pk_bf16_f32 v240, v68, v76\n\t"
    "v_cvt_pk_bf16_f32 v238, v69, v77\n\t"
    "v_mfma_f32_16x16x32_bf16 v[176:179], v[100:103], v[148:151], 0\n\t"
    "v_cvt_pk_bf16_f32 v236, v70, v78\n\t"
    "v_cvt_pk_bf16_f32 v234, v71, v79\n\t"
    "v_mfma_f32_16x16x32_bf16 v[176:179], v[64:67], v[132:135], v[176:179]\n\t"
    "v_cvt_pk_bf16_f32 v241, v144, v152\n\t"
    "v_cvt_pk_bf16_f32 v239, v145, v153\n\t"
    "v_mfma_f32_16x16x32_bf16 v[180:183], v[100:103], v[72:75], 0\n\t"
    "buffer_store_dwordx2 v[240:241], v24, s[12:15], s51 offen offset:0 sc1\n\t"
    "v_mfma_f32_16x16x32_bf16 v[180:183], v[64:67], v[116:119], v[180:183]\n\t"
    "v_cvt_pk_bf16_f32 v237, v146, v154\n\t"
    "v_cvt_pk_bf16_f32 v235, v147, v155\n\t"
    "v_mfma_f32_16x16x32_bf16 v[68:71], v[100:103], v[164:167], 0\n\t"
    "v_cvt_pk_bf16_f32 v242, v92, v176\n\t"
    "v_cvt_pk_bf16_f32 v241, v93, v177\n\t"
    "v_mfma_f32_16x16x32_bf16 v[68:71], v[64:67], v[120:123], v[68:71]\n\t"
    "buffer_store_dword v242, v251, s[12:15], s51 offen offset:128 sc1\n\t"
    "v_cvt_pk_bf16_f32 v240, v94, v178\n\t"
    "v_mfma_f32_16x16x32_bf16 v[88:91], a[56:59], a[136:139], v[88:91]\n\t"
    "v_cvt_pk_bf16_f32 v233, v95, v179\n\t"
    "v_mfma_f32_16x16x32_bf16 v[172:175], a[120:123], a[92:95], v[172:175]\n\t"
    "buffer_store_dword v241, v252, s[12:15], s51 offen offset:512 sc1\n\t"
    "s_waitcnt lgkmcnt(2)\n\t"
    "v_mfma_f32_16x16x32_bf16 v[156:159], a[12:15], a[140:143], v[156:159]\n\t"
    "v_cvt_pk_bf16_f32 v242, v180, v68\n\t"
    "v_cvt_pk_bf16_f32 v232, v181, v69\n\t"
    "v_mfma_f32_16x16x32_bf16 v[168:171], a[36:39], a[140:143], v[168:171]\n\t"
    "buffer_store_dword v240, v253, s[12:15], s51 offen offset:896 sc1\n\t"
    "v_cvt_pk_bf16_f32 v241, v182, v70\n\t"
    "v_mfma_f32_16x16x32_bf16 v[76:79], a[156:159], a[72:75], 0\n\t"
    "v_cvt_pk_bf16_f32 v230, v172, v173\n\t"
    "v_cvt_pk_bf16_f32 v231, v174, v175\n\t"
    "v_mfma_f32_16x16x32_bf16 v[88:91], a[60:63], a[140:143], v[88:91]\n\t"
    "buffer_store_dword v233, v254, s[12:15], s51 offen offset:1280 sc1\n\t"
    "v_cvt_pk_bf16_f32 v240, v183, v71\n\t"
    "v_mfma_f32_16x16x32_bf16 v[76:79], a[160:163], a[76:79], v[76:79]\n\t"
    "ds_write_b64 v32, v[230:231] offset:512\n\t"
    "v_mfma_f32_16x16x32_bf16 v[156:159], a[16:19], a[152:155], v[156:159]\n\t"
    "buffer_store_dword v242, v251, s[12:15], s51 offen offset:192 sc1\n\t"
    "v_mfma_f32_16x16x32_bf16 v[168:171], a[40:43], a[152:155], v[168:171]\n\t"
    "ds_read_b128 a[120:123], v8 offset:44032\n\t"
    "ds_read_b128 a[132:135], v8 offset:57344\n\t"
    "v_mfma_f32_16x16x32_bf16 v[76:79], a[96:99], a[80:83], v[76:79]\n\t"
    "buffer_store_dword v232, v252, s[12:15], s51 offen offset:576 sc1\n\t"
    "v_mfma_f32_16x16x32_bf16 v[88:91], a[64:67], a[152:155], v[88:91]\n\t"
    "ds_read_b128 a[136:139], v8 offset:58368\n\t"
    "v_mfma_f32_16x16x32_bf16 v[76:79], a[112:115], a[84:87], v[76:79]\n\t"
    "buffer_store_dword v241, v253, s[12:15], s51 offen offset:960 sc1\n\t"
    "v_mfma_f32_16x16x32_bf16 v[156:159], a[20:23], a[164:167], v[156:159]\n\t"
    "ds_read_b128 a[96:99], v8 offset:59392\n\t"
    "v_mfma_f32_16x16x32_bf16 v[168:171], a[44:47], a[164:167], v[168:171]\n\t"
    "buffer_store_dword v240, v254, s[12:15], s51 offen offset:1344 sc1\n\t"
    "v_mfma_f32_16x16x32_bf16 v[76:79], a[168:171], a[88:91], v[76:79]\n\t"
    "ds_read_b128 a[112:115], v8 offset:60416\n\t"
    "v_mfma_f32_16x16x32_bf16 v[88:91], a[68:71], a[164:167], v[88:91]\n\t"
    "v_cvt_pk_bf16_f32 v68, v156, v157\n\t"
    "v_cvt_pk_bf16_f32 v69, v158, v159\n\t"
    "v_mfma_f32_16x16x32_bf16 v[76:79], a[100:103], a[92:95], v[76:79]\n\t"
    "v_cvt_pk_bf16_f32 v70, v168, v169\n\t"
    "v_cvt_pk_bf16_f32 v71, v170, v171\n\t"
    "v_mfma_f32_16x16x32_bf16 v[92:95], a[0:3], a[108:111], 0\n\t"
    "buffer_store_dwordx2 v[238:239], v25, s[12:15], s51 offen offset:384 sc1\n\t"
    "v_mfma_f32_16x16x32_bf16 v[144:147], v[112:115], v[68:71], 0\n\t"
    "v_cvt_pk_bf16_f32 v160, v88, v89\n\t"
    "v_cvt_pk_bf16_f32 v161, v90, v91\n\t"
    "v_mfma_f32_16x16x32_bf16 v[152:155], v[96:99], v[68:71], 0\n\t"
    "v_cvt_pk_bf16_f32 v240, v76, v77\n\t"
    "v_cvt_pk_bf16_f32 v241, v78, v79\n\t"
    "v_mfma_f32_16x16x32_bf16 v[144:147], v[52:55], v[160:163], v[144:147]\n\t"
    "ds_write_b64 v32, v[240:241] offset:1024\n\t"
    "v_mfma_f32_16x16x32_bf16 v[152:155], v[56:59], v[160:163], v[152:155]\n\t"
    "buffer_store_dwordx2 v[236:237], v26, s[12:15], s51 offen offset:768 sc1\n\t"
    "v_mfma_f32_16x16x32_bf16 v[76:79], v[100:103], v[68:71], 0\n\t"
    "ds_read_b128 a[100:103], v8 offset:61440\n\t"
    "s_waitcnt lgkmcnt(4)\n\t"
    "v_mfma_f32_16x16x32_bf16 v[76:79], v[64:67], v[160:163], v[76:79]\n\t"
    "v_mfma_f32_16x16x32_bf16 v[88:91], a[24:27], a[108:111], 0\n\t"
    "ds_read2_b32 v[158:159], v31 offset0:144 offset1:144\n\t"
    "v_mfma_f32_16x16x32_bf16 v[168:171], a[48:51], a[108:111], 0\n\t"
    "buffer_store_dwordx2 v[234:235], v27, s[12:15], s51 offen offset:1152 sc1\n\t"
    "v_mfma_f32_16x16x32_bf16 v[92:95], a[4:7], a[104:107], v[92:95]\n\t"
    "ds_read_b128 a[140:143], v8 offset:62464\n\t"
    "v_mfma_f32_16x16x32_bf16 v[88:91], a[28:31], a[104:107], v[88:91]\n\t"
    "v_mfma_f32_16x16x32_bf16 v[168:171], a[52:55], a[104:107], v[168:171]\n\t"
    "ds_read_b128 a[108:111], v9 offset:10240\n\t"
    "v_mfma_f32_16x16x32_bf16 v[92:95], a[8:11], a[116:119], v[92:95]\n\t"
    "v_mfma_f32_16x16x32_bf16 v[88:91], a[32:35], a[116:119], v[88:91]\n\t"
    "s_waitcnt lgkmcnt(4)\n\t"
    "s_barrier\n\t"
    "v_mfma_f32_16x16x32_bf16 v[168:171], a[56:59], a[116:119], v[168:171]\n\t"
    "ds_read_b128 a[104:107], v29 offset:0\n\t"
    "ds_read_b128 a[144:147], v29 offset:256\n\t"
    "v_mfma_f32_16x16x32_bf16 v[92:95], a[12:15], a[124:127], v[92:95]\n\t"
    "ds_read_b128 a[148:151], v29 offset:512\n\t"
    "v_mfma_f32_16x16x32_bf16 v[88:91], a[36:39], a[124:127], v[88:91]\n\t"
    "ds_read_b128 a[116:119], v29 offset:768\n\t"
    "v_mfma_f32_16x16x32_bf16 v[168:171], a[60:63], a[124:127], v[168:171]\n\t"
    "ds_read_b128 a[152:155], v29 offset:1024\n\t"
    "v_mfma_f32_16x16x32_bf16 v[92:95], a[16:19], a[128:131], v[92:95]\n\t"
    "ds_read_b128 a[156:159], v29 offset:1280\n\t"
    "v_mfma_f32_16x16x32_bf16 v[88:91], a[40:43], a[128:131], v[88:91]\n\t"
    "ds_read_b128 a[124:127], v9 offset:11264\n\t"
    "v_mfma_f32_16x16x32_bf16 v[168:171], a[64:67], a[128:131], v[168:171]\n\t"
    "ds_read_b128 a[160:163], v9 offset:12288\n\t"
    "v_mfma_f32_16x16x32_bf16 v[92:95], a[20:23], a[120:123], v[92:95]\n\t"
    "ds_read_b128 a[164:167], v9 offset:13312\n\t"
    "v_mfma_f32_16x16x32_bf16 v[88:91], a[44:47], a[120:123], v[88:91]\n\t"
    "v_mfma_f32_16x16x32_bf16 v[168:171], a[68:71], a[120:123], v[168:171]\n\t"
    "ds_read_b128 a[128:131], v9 offset:14336\n\t"
    "s_waitcnt lgkmcnt(9)\n\t"
    "v_mfma_f32_16x16x32_bf16 v[172:175], a[104:107], a[0:3], 0\n\t"
    "v_cvt_pk_bf16_f32 v176, v92, v93\n\t"
    "v_cvt_pk_bf16_f32 v177, v94, v95\n\t"
    "v_mfma_f32_16x16x32_bf16 v[180:183], a[104:107], a[24:27], 0\n\t"
    "v_cvt_pk_bf16_f32 v178, v88, v89\n\t"
    "v_cvt_pk_bf16_f32 v179, v90, v91\n\t"
    "v_mfma_f32_16x16x32_bf16 v[184:187], a[104:107], a[48:51], 0\n\t"
    "v_cvt_pk_bf16_f32 v156, v168, v169\n\t"
    "v_cvt_pk_bf16_f32 v157, v170, v171\n\t"
    "v_mfma_f32_16x16x32_bf16 v[92:95], v[112:115], v[176:179], 0\n\t"
    "v_mfma_f32_16x16x32_bf16 v[88:91], v[96:99], v[176:179], 0\n\t"
    "v_mfma_f32_16x16x32_bf16 v[92:95], v[52:55], v[156:159], v[92:95]\n\t"
    "v_mfma_f32_16x16x32_bf16 v[88:91], v[56:59], v[156:159], v[88:91]\n\t"
    "s_waitcnt lgkmcnt(5)\n\t"
    "v_mfma_f32_16x16x32_bf16 v[168:171], v[100:103], v[176:179], 0\n\t"
    "v_mfma_f32_16x16x32_bf16 v[168:171], v[64:67], v[156:159], v[168:171]\n\t"
    "v_cvt_pk_bf16_f32 v242, v144, v92\n\t"
    "v_cvt_pk_bf16_f32 v241, v145, v93\n\t"
    "v_mfma_f32_16x16x32_bf16 v[188:191], a[104:107], a[72:75], 0\n\t"
    "buffer_store_dword v242, v243, s[12:15], s49 offen offset:256 sc1\n\t"
    "v_cvt_pk_bf16_f32 v240, v146, v94\n\t"
    "v_mfma_f32_16x16x32_bf16 v[172:175], a[144:147], a[4:7], v[172:175]\n\t"
    "v_cvt_pk_bf16_f32 v239, v147, v95\n\t"
    "v_cvt_pk_bf16_f32 v238, v152, v88\n\t"
    "v_mfma_f32_16x16x32_bf16 v[180:183], a[144:147], a[28:31], v[180:183]\n\t"
    "buffer_store_dword v241, v244, s[12:15], s49 offen offset:640 sc1\n\t"
    "v_cvt_pk_bf16_f32 v242, v153, v89\n\t"
    "v_mfma_f32_16x16x32_bf16 v[184:187], a[144:147], a[52:55], v[184:187]\n\t"
    "v_cvt_pk_bf16_f32 v237, v154, v90\n\t"
    "v_cvt_pk_bf16_f32 v236, v155, v91\n\t"
    "v_mfma_f32_16x16x32_bf16 v[188:191], a[144:147], a[76:79], v[188:191]\n\t"
    "buffer_store_dword v240, v245, s[12:15], s49 offen offset:1024 sc1\n\t"
    "v_cvt_pk_bf16_f32 v241, v76, v168\n\t"
    "v_mfma_f32_16x16x32_bf16 v[172:175], a[148:151], a[8:11], v[172:175]\n\t"
    "v_cvt_pk_bf16_f32 v235, v77, v169\n\t"
    "v_cvt_pk_bf16_f32 v234, v78, v170\n\t"
    "v_mfma_f32_16x16x32_bf16 v[180:183], a[148:151], a[32:35], v[180:183]\n\t"
    "buffer_store_dword v239, v246, s[12:15], s49 offen offset:1408 sc1\n\t"
    "v_cvt_pk_bf16_f32 v240, v79, v171\n\t"
    "v_mfma_f32_16x16x32_bf16 v[184:187], a[148:151], a[56:59], v[184:187]\n\t"
    "s_waitcnt lgkmcnt(0)\n\t"
    "v_mfma_f32_16x16x32_bf16 v[188:191], a[148:151], a[80:83], v[188:191]\n\t"
    "buffer_store_dword v238, v247, s[12:15], s50 offen offset:256 sc1\n\t"
    "v_mfma_f32_16x16x32_bf16 v[172:175], a[116:119], a[12:15], v[172:175]\n\t"
    "v_mfma_f32_16x16x32_bf16 v[180:183], a[116:119], a[36:39], v[180:183]\n\t"
    "buffer_store_dword v242, v248, s[12:15], s50 offen offset:640 sc1\n\t"
    "v_mfma_f32_16x16x32_bf16 v[184:187], a[116:119], a[60:63], v[184:187]\n\t"
    "v_mfma_f32_16x16x32_bf16 v[188:191], a[116:119], a[84:87], v[188:191]\n\t"
    "buffer_store_dword v237, v249, s[12:15], s50 offen offset:1024 sc1\n\t"
    "v_mfma_f32_16x16x32_bf16 v[172:175], a[152:155], a[16:19], v[172:175]\n\t"
    "v_mfma_f32_16x16x32_bf16 v[180:183], a[152:155], a[40:43], v[180:183]\n\t"
    "buffer_store_dword v236, v250, s[12:15], s50 offen offset:1408 sc1\n\t"
    "v_mfma_f32_16x16x32_bf16 v[184:187], a[152:155], a[64:67], v[184:187]\n\t"
    "v_mfma_f32_16x16x32_bf16 v[188:191], a[152:155], a[88:91], v[188:191]\n\t"
    "buffer_store_dword v241, v251, s[12:15], s51 offen offset:256 sc1\n\t"
    "v_mfma_f32_16x16x32_bf16 v[172:175], a[156:159], a[20:23], v[172:175]\n\t"
    "v_mfma_f32_16x16x32_bf16 v[180:183], a[156:159], a[44:47], v[180:183]\n\t"
    "buffer_store_dword v235, v252, s[12:15], s51 offen offset:640 sc1\n\t"
    "v_mfma_f32_16x16x32_bf16 v[184:187], a[156:159], a[68:71], v[184:187]\n\t"
    "v_mfma_f32_16x16x32_bf16 v[188:191], a[156:159], a[92:95], v[188:191]\n\t"
    "buffer_store_dword v234, v253, s[12:15], s51 offen offset:1024 sc1\n\t"
    "v_mfma_f32_16x16x32_bf16 v[76:79], a[0:3], a[132:135], 0\n\t"
    "v_mfma_f32_16x16x32_bf16 v[88:91], a[24:27], a[132:135], 0\n\t"
    "buffer_store_dword v240, v254, s[12:15], s51 offen offset:1408 sc1\n\t"
    "v_mfma_f32_16x16x32_bf16 v[92:95], a[48:51], a[132:135], 0\n\t"
    "ds_read2_b32 v[146:147], v31 offset0:160 offset1:160\n\t"
    "v_cndmask_b32_e64 v242, v35, v188, s[40:41]\n\t"
    "v_mfma_f32_16x16x32_bf16 v[76:79], a[4:7], a[136:139], v[76:79]\n\t"
    "ds_read_b128 a[104:107], v9 offset:15360\n\t"
    "v_cndmask_b32_e64 v242, v242, v172, s[22:23]\n\t"
    "v_mfma_f32_16x16x32_bf16 v[88:91], a[28:31], a[136:139], v[88:91]\n\t"
    "v_cndmask_b32_e64 v242, v242, v180, s[36:37]\n\t"
    "v_cndmask_b32_e64 v242, v242, v184, s[38:39]\n\t"
    "v_mfma_f32_16x16x32_bf16 v[92:95], a[52:55], a[136:139], v[92:95]\n\t"
    "v_mfma_f32_16x16x32_bf16 v[76:79], a[8:11], a[96:99], v[76:79]\n\t"
    "v_max_f32_dpp v241, v242, v242 row_ror:8 row_mask:0xf bank_mask:0xf\n\t"
    "v_mfma_f32_16x16x32_bf16 v[88:91], a[32:35], a[96:99], v[88:91]\n\t"
    "v_mfma_f32_16x16x32_bf16 v[92:95], a[56:59], a[96:99], v[92:95]\n\t"
    "v_max_f32_dpp v241, v241, v241 row_ror:4 row_mask:0xf bank_mask:0xf\n\t"
    "v_mfma_f32_16x16x32_bf16 v[76:79], a[12:15], a[112:115], v[76:79]\n\t"
    "v_mfma_f32_16x16x32_bf16 v[88:91], a[36:39], a[112:115], v[88:91]\n\t"
    "v_max_f32_dpp v241, v241, v241 row_ror:2 row_mask:0xf bank_mask:0xf\n\t"
    "v_mfma_f32_16x16x32_bf16 v[92:95], a[60:63], a[112:115], v[92:95]\n\t"
    "v_mfma_f32_16x16x32_bf16 v[76:79], a[16:19], a[100:103], v[76:79]\n\t"
    "v_max_f32_dpp v241, v241, v241 row_ror:1 row_mask:0xf bank_mask:0xf\n\t"
    "v_mov_b32 v240, v241\n\t"
    "v_mfma_f32_16x16x32_bf16 v[88:91], a[40:43], a[100:103], v[88:91]\n\t"
    "v_mfma_f32_16x16x32_bf16 v[92:95], a[64:67], a[100:103], v[92:95]\n\t"
    "v_permlane32_swap_b32 v241, v240\n\t"
    "v_mfma_f32_16x16x32_bf16 v[76:79], a[20:23], a[140:143], v[76:79]\n\t"
    "v_max_f32 v241, v241, v240\n\t"
    "v_mov_b32 v240, v241\n\t"
    "v_mfma_f32_16x16x32_bf16 v[88:91], a[44:47], a[140:143], v[88:91]\n\t"
    "v_mfma_f32_16x16x32_bf16 v[92:95], a[68:71], a[140:143], v[92:95]\n\t"
    "v_permlane16_swap_b32 v241, v240\n\t"
    "v_mfma_f32_16x16x32_bf16 v[152:155], a[0:3], a[108:111], 0\n\t"
    "v_max_f32 v241, v241, v240\n\t"
    "v_sub_f32 v242, v242, v241\n\t"
    "v_mfma_f32_16x16x32_bf16 v[168:171], a[24:27], a[108:111], 0\n\t"
    "v_exp_f32 v242, v242\n\t"
    "v_mfma_f32_16x16x32_bf16 v[172:175], a[48:51], a[108:111], 0\n\t"
    "v_cvt_pk_bf16_f32 v180, v76, v77\n\t"
    "v_add_f32_dpp v241, v242, v242 row_ror:8 row_mask:0xf bank_mask:0xf\n\t"
    "v_mfma_f32_16x16x32_bf16 v[152:155], a[4:7], a[124:127], v[152:155]\n\t"
    "v_cvt_pk_bf16_f32 v181, v78, v79\n\t"
    "v_add_f32_dpp v241, v241, v241 row_ror:4 row_mask:0xf bank_mask:0xf\n\t"
    "v_mfma_f32_16x16x32_bf16 v[168:171], a[28:31], a[124:127], v[168:171]\n\t"
    "v_cvt_pk_bf16_f32 v182, v88, v89\n\t"
    "v_add_f32_dpp v241, v241, v241 row_ror:2 row_mask:0xf bank_mask:0xf\n\t"
    "v_mfma_f32_16x16x32_bf16 v[172:175], a[52:55], a[124:127], v[172:175]\n\t"
    "v_cvt_pk_bf16_f32 v183, v90, v91\n\t"
    "v_add_f32_dpp v241, v241, v241 row_ror:1 row_mask:0xf bank_mask:0xf\n\t"
    "v_mfma_f32_16x16x32_bf16 v[152:155], a[8:11], a[160:163], v[152:155]\n\t"
    "v_mov_b32 v240, v241\n\t"
    "v_cvt_pk_bf16_f32 v144, v92, v93\n\t"
    "Lst3%=:\n\t"
    "s_cbranch_execz Lstret%=\n\t"
    "v_mfma_f32_16x16x32_bf16 v[168:171], a[32:35], a[160:163], v[168:171]\n\t"
    "v_permlane32_swap_b32 v241, v240\n\t"
    "v_mfma_f32_16x16x32_bf16 v[172:175], a[56:59], a[160:163], v[172:175]\n\t"
    "v_add_f32 v241, v241, v240\n\t"
    "v_mov_b32 v240, v241\n\t"
    "v_mfma_f32_16x16x32_bf16 v[152:155], a[12:15], a[164:167], v[152:155]\n\t"
    "ds_read2_b32 v[78:79], v31 offset0:176 offset1:176\n\t"
    "v_cvt_pk_bf16_f32 v145, v94, v95\n\t"
    "v_mfma_f32_16x16x32_bf16 v[168:171], a[36:39], a[164:167], v[168:171]\n\t"
    "v_permlane16_swap_b32 v241, v240\n\t"
    "v_mfma_f32_16x16x32_bf16 v[172:175], a[60:63], a[164:167], v[172:175]\n\t"
    "v_add_f32 v241, v241, v240\n\t"
    "v_mfma_f32_16x16x32_bf16 v[152:155], a[16:19], a[128:131], v[152:155]\n\t"
    "v_rcp_f32 v241, v241\n\t"
    "v_mfma_f32_16x16x32_bf16 v[168:171], a[40:43], a[128:131], v[168:171]\n\t"
    "v_mul_f32 v242, v242, v241\n\t"
    "v_cvt_pk_bf16_f32 v242, v242, v242\n\t"
    "v_mfma_f32_16x16x32_bf16 v[172:175], a[64:67], a[128:131], v[172:175]\n\t"
    "s_waitcnt lgkmcnt(1)\n\t"
    "v_mfma_f32_16x16x32_bf16 v[152:155], a[20:23], a[104:107], v[152:155]\n\t"
    "ds_write_b16 v37, v242\n\t"
    "v_mfma_f32_16x16x32_bf16 v[168:171], a[44:47], a[104:107], v[168:171]\n\t"
    "ds_read_b128 a[96:99], v38 offset:0\n\t"
    "ds_read_b128 a[100:103], v38 offset:64\n\t"
    "v_mfma_f32_16x16x32_bf16 v[172:175], a[68:71], a[104:107], v[172:175]\n\t"
    "v_mfma_f32_16x16x32_bf16 v[88:91], v[112:115], v[180:183], 0\n\t"
    "v_cvt_pk_bf16_f32 v92, v152, v153\n\t"
    "v_cvt_pk_bf16_f32 v93, v154, v155\n\t"
    "v_mfma_f32_16x16x32_bf16 v[88:91], v[52:55], v[144:147], v[88:91]\n\t"
    "v_cvt_pk_bf16_f32 v94, v168, v169\n\t"
    "v_cvt_pk_bf16_f32 v95, v170, v171\n\t"
    "v_mfma_f32_16x16x32_bf16 v[184:187], v[96:99], v[180:183], 0\n\t"
    "v_cvt_pk_bf16_f32 v76, v172, v173\n\t"
    "v_cvt_pk_bf16_f32 v77, v174, v175\n\t"
    "v_mfma_f32_16x16x32_bf16 v[152:155], v[112:115], v[92:95], 0\n\t"
    "v_mfma_f32_16x16x32_bf16 v[184:187], v[56:59], v[144:147], v[184:187]\n\t"
    "s_waitcnt lgkmcnt(3)\n\t"
    "v_mfma_f32_16x16x32_bf16 v[152:155], v[52:55], v[76:79], v[152:155]\n\t"
    "v_mfma_f32_16x16x32_bf16 v[168:171], v[96:99], v[92:95], 0\n\t"
    "v_mfma_f32_16x16x32_bf16 v[168:171], v[56:59], v[76:79], v[168:171]\n\t"
    "v_mfma_f32_16x16x32_bf16 v[112:115], v[100:103], v[180:183], 0\n\t"
    "v_cvt_pk_bf16_f32 v242, v88, v152\n\t"
    "v_cvt_pk_bf16_f32 v241, v89, v153\n\t"
    "v_mfma_f32_16x16x32_bf16 v[112:115], v[64:67], v[144:147], v[112:115]\n\t"
    "buffer_store_dword v242, v243, s[12:15], s49 offen offset:320 sc1\n\t"
    "v_cvt_pk_bf16_f32 v240, v90, v154\n\t"
    "v_mfma_f32_16x16x32_bf16 v[52:55], v[100:103], v[92:95], 0\n\t"
    "v_cvt_pk_bf16_f32 v239, v91, v155\n\t"
    "v_cvt_pk_bf16_f32 v238, v184, v168\n\t"
    "v_mfma_f32_16x16x32_bf16 v[52:55], v[64:67], v[76:79], v[52:55]\n\t"
    "buffer_store_dword v241, v244, s[12:15], s49 offen offset:704 sc1\n\t"
    "v_cvt_pk_bf16_f32 v242, v185, v169\n\t"
    "s_waitcnt lgkmcnt(0)\n\t"
    "v_mfma_f32_16x16x32_bf16 v[56:59], a[96:99], v[80:83], 0\n\t"
    "v_cvt_pk_bf16_f32 v237, v186, v170\n\t"
    "v_cvt_pk_bf16_f32 v241, v187, v171\n\t"
    "v_mfma_f32_16x16x32_bf16 v[56:59], a[100:103], v[136:139], v[56:59]\n\t"
    "buffer_store_dword v240, v245, s[12:15], s49 offen offset:1088 sc1\n\t"
    "v_cvt_pk_bf16_f32 v236, v112, v52\n\t"
    "v_mfma_f32_16x16x32_bf16 v[64:67], a[96:99], v[104:107], 0\n\t"
    "v_cvt_pk_bf16_f32 v235, v113, v53\n\t"
    "v_cvt_pk_bf16_f32 v234, v114, v54\n\t"
    "v_mfma_f32_16x16x32_bf16 v[64:67], a[100:103], v[124:127], v[64:67]\n\t"
    "buffer_store_dword v239, v246, s[12:15], s49 offen offset:1472 sc1\n\t"
    "v_cvt_pk_bf16_f32 v240, v115, v55\n\t"
    "v_mfma_f32_16x16x32_bf16 v[80:83], a[96:99], v[128:131], 0\n\t"
    "v_mfma_f32_16x16x32_bf16 v[80:83], a[100:103], v[140:143], v[80:83]\n\t"
    "buffer_store_dword v238, v247, s[12:15], s50 offen offset:320 sc1\n\t"
    "v_mfma_f32_16x16x32_bf16 v[88:91], a[96:99], v[44:47], 0\n\t"
    "v_cvt_pk_bf16_f32 v232, v56, v64\n\t"
    "v_mfma_f32_16x16x32_bf16 v[88:91], a[100:103], v[108:111], v[88:91]\n\t"
    "buffer_store_dword v242, v248, s[12:15], s50 offen offset:704 sc1\n\t"
    "v_mfma_f32_16x16x32_bf16 v[52:55], a[96:99], v[84:87], 0\n\t"
    "v_mfma_f32_16x16x32_bf16 v[52:55], a[100:103], v[40:43], v[52:55]\n\t"
    "buffer_store_dword v237, v249, s[12:15], s50 offen offset:1088 sc1\n\t"
    "v_mfma_f32_16x16x32_bf16 v[44:47], a[96:99], v[148:151], 0\n\t"
    "v_cvt_pk_bf16_f32 v233, v80, v88\n\t"
    "v_mfma_f32_16x16x32_bf16 v[44:47], a[100:103], v[132:135], v[44:47]\n\t"
    "buffer_store_dword v241, v250, s[12:15], s50 offen offset:1472 sc1\n\t"
    "v_mfma_f32_16x16x32_bf16 v[40:43], a[96:99], v[72:75], 0\n\t"
    "buffer_store_dwordx2 v[232:233], v28, s[12:15], s49 offen offset:0 sc1\n\t"
    "v_mfma_f32_16x16x32_bf16 v[40:43], a[100:103], v[116:119], v[40:43]\n\t"
    "buffer_store_dword v236, v251, s[12:15], s51 offen offset:320 sc1\n\t"
    "v_mfma_f32_16x16x32_bf16 v[56:59], a[96:99], v[164:167], 0\n\t"
    "v_cvt_pk_bf16_f32 v242, v52, v44\n\t"
    "v_mfma_f32_16x16x32_bf16 v[56:59], a[100:103], v[120:123], v[56:59]\n\t"
    "buffer_store_dword v235, v252, s[12:15], s51 offen offset:704 sc1\n\t"
    "v_mfma_f32_16x16x32_bf16 v[64:67], a[96:99], v[68:71], 0\n\t"
    "buffer_store_dword v242, v255, s[12:15], s49 offen offset:128 sc1\n\t"
    "v_mfma_f32_16x16x32_bf16 v[64:67], a[100:103], v[160:163], v[64:67]\n\t"
    "buffer_store_dword v234, v253, s[12:15], s51 offen offset:1088 sc1\n\t"
    "v_mfma_f32_16x16x32_bf16 v[44:47], a[96:99], v[176:179], 0\n\t"
    "v_cvt_pk_bf16_f32 v241, v40, v56\n\t"
    "v_mfma_f32_16x16x32_bf16 v[44:47], a[100:103], v[156:159], v[44:47]\n\t"
    "buffer_store_dword v240, v254, s[12:15], s51 offen offset:1472 sc1\n\t"
    "v_mfma_f32_16x16x32_bf16 v[52:55], a[96:99], v[180:183], 0\n\t"
    "buffer_store_dword v241, v255, s[12:15], s49 offen offset:192 sc1\n\t"
    "v_mfma_f32_16x16x32_bf16 v[52:55], a[100:103], v[144:147], v[52:55]\n\t"
    "v_mfma_f32_16x16x32_bf16 v[40:43], a[96:99], v[92:95], 0\n\t"
    "v_cvt_pk_bf16_f32 v242, v64, v44\n\t"
    "v_mfma_f32_16x16x32_bf16 v[40:43], a[100:103], v[76:79], v[40:43]\n\t"
    "buffer_store_dword v242, v255, s[12:15], s49 offen offset:256 sc1\n\t"
    "s_nop 6\n\t"
    "v_cvt_pk_bf16_f32 v241, v52, v40\n\t"
    "buffer_store_dword v241, v255, s[12:15], s49 offen offset:320 sc1\n\t"
    "s_endpgm\n\t"
    "Lfslow%=:\n\t"
    "buffer_load_dwordx4 v[112:115], v0, s[16:19], s61 offen sc1\n\t"
    "s_waitcnt vmcnt(0)\n\t"
    "v_cmp_eq_u64 s[46:47], s[44:45], v[112:113]\n\t"
    "v_cmp_eq_u64 s[62:63], s[44:45], v[114:115]\n\t"
    "s_sub_u32 s58, s58, 1\n\t"
    "s_cmp_lt_i32 s58, 1\n\t"
    "s_cbranch_scc1 Lfback%=\n\t"
    "s_and_b64 s[46:47], s[46:47], s[62:63]\n\t"
    "s_andn2_b64 s[46:47], s[56:57], s[46:47]\n\t"
    "s_cbranch_scc1 Lfslow%=\n\t"
    "s_branch Lfback%=\n\t"
    "Lchk%=:\n\t"
    "v_cmp_eq_u64 vcc, s[44:45], v[96:97]\n\t"
    "v_cmp_eq_u64 s[46:47], s[44:45], v[98:99]\n\t"
    "v_cmp_eq_u64 s[54:55], s[44:45], v[116:117]\n\t"
    "v_cmp_eq_u64 s[62:63], s[44:45], v[118:119]\n\t"
    "s_nop 1\n\t"
    "s_and_b64 vcc, vcc, s[46:47]\n\t"
    "s_and_b64 s[54:55], s[54:55], s[62:63]\n\t"
    "s_and_b64 vcc, vcc, s[54:55]\n\t"
    "s_cmp_eq_u64 vcc, -1\n\t"
    "s_cbranch_scc1 Lok%=\n\t"
    "s_sub_u32 s58, s58, 1\n\t"
    "s_cmp_lt_i32 s58, 1\n\t"
    "s_cbranch_scc1 Lok%=\n\t"
    "buffer_load_dwordx4 v[96:99], v1, s[16:19], s60 offen offset:0 sc1\n\t"
    "buffer_load_dwordx4 v[116:119], v1, s[16:19], s60 offen offset:1024 sc1\n\t"
    "s_waitcnt vmcnt(0)\n\t"
    "s_branch Lchk%=\n\t"
    "Lok%=:\n\t"
    "v_mov_b32 v96, s44\n\t"
    "v_mov_b32 v97, s45\n\t"
    "v_mov_b32 v98, 158720\n\t"
    "buffer_store_dwordx2 v[96:97], v98, s[16:19], 0 offen sc1\n\t"
    "s_nop 1\n\t"
    "s_branch Laback%=\n\t"
    "Lend%=:\n\t"
    : "+{v0}"(tid), "+{s[0:1]}"(karg)
    : "{s34}"(bid)
    : "v1", "v2", "v3", "v4", "v5", "v6", "v7", "v8", "v9", "v10", "v11", "v12", "v13", "v14", "v15", "v16", "v17", "v18", "v19", "v20", "v21", "v22", "v23", "v24", "v25", "v26", "v27", "v28", "v29", "v30", "v31", "v32", "v33", "v34", "v35", "v36", "v37", "v38", "v39", "v40", "v41", "v42", "v43", "v44", "v45", "v46", "v47", "v48", "v49", "v50", "v51", "v52", "v53", "v54", "v55", "v56", "v57", "v58", "v59", "v60", "v61", "v62", "v63", "v64", "v65", "v66", "v67", "v68", "v69", "v70", "v71", "v72", "v73", "v74", "v75", "v76", "v77", "v78", "v79", "v80", "v81", "v82", "v83", "v84", "v85", "v86", "v87", "v88", "v89", "v90", "v91", "v92", "v93", "v94", "v95", "v96", "v97", "v98", "v99", "v100", "v101", "v102", "v103", "v104", "v105", "v106", "v107", "v108", "v109", "v110", "v111", "v112", "v113", "v114", "v115", "v116", "v117", "v118", "v119", "v120", "v121", "v122", "v123", "v124", "v125", "v126", "v127", "v128", "v129", "v130", "v131", "v132", "v133", "v134", "v135", "v136", "v137", "v138", "v139", "v140", "v141", "v142", "v143", "v144", "v145", "v146", "v147", "v148", "v149", "v150", "v151", "v152", "v153", "v154", "v155", "v156", "v157", "v158", "v159", "v160", "v161", "v162", "v163", "v164", "v165", "v166", "v167", "v168", "v169", "v170", "v171", "v172", "v173", "v174", "v175", "v176", "v177", "v178", "v179", "v180", "v181", "v182", "v183", "v184", "v185", "v186", "v187", "v188", "v189", "v190", "v191", "v192", "v193", "v194", "v195", "v196", "v197", "v198", "v199", "v200", "v201", "v202", "v203", "v204", "v205", "v206", "v207", "v208", "v209", "v210", "v211", "v212", "v213", "v214", "v215", "v216", "v217", "v218", "v219", "v220", "v221", "v222", "v223", "v224", "v225", "v226", "v227", "v228", "v229", "v230", "v231", "v232", "v233", "v234", "v235", "v236", "v237", "v238", "v239", "v240", "v241", "v242", "v243", "v244", "v245", "v246", "v247", "v248", "v249", "v250", "v251", "v252", "v253", "v254", "v255", "a0", "a1", "a2", "a3", "a4", "a5", "a6", "a7", "a8", "a9", "a10", "a11", "a12", "a13", "a14", "a15", "a16", "a17", "a18", "a19", "a20", "a21", "a22", "a23", "a24", "a25", "a26", "a27", "a28", "a29", "a30", "a31", "a32", "a33", "a34", "a35", "a36", "a37", "a38", "a39", "a40", "a41", "a42", "a43", "a44", "a45", "a46", "a47", "a48", "a49", "a50", "a51", "a52", "a53", "a54", "a55", "a56", "a57", "a58", "a59", "a60", "a61", "a62", "a63", "a64", "a65", "a66", "a67", "a68", "a69", "a70", "a71", "a72", "a73", "a74", "a75", "a76", "a77", "a78", "a79", "a80", "a81", "a82", "a83", "a84", "a85", "a86", "a87", "a88", "a89", "a90", "a91", "a92", "a93", "a94", "a95", "a96", "a97", "a98", "a99", "a100", "a101", "a102", "a103", "a104", "a105", "a106", "a107", "a108", "a109", "a110", "a111", "a112", "a113", "a114", "a115", "a116", "a117", "a118", "a119", "a120", "a121", "a122", "a123", "a124", "a125", "a126", "a127", "a128", "a129", "a130", "a131", "a132", "a133", "a134", "a135", "a136", "a137", "a138", "a139", "a140", "a141", "a142", "a143", "a144", "a145", "a146", "a147", "a148", "a149", "a150", "a151", "a152", "a153", "a154", "a155", "a156", "a157", "a158", "a159", "a160", "a161", "a162", "a163", "a164", "a165", "a166", "a167", "a168", "a169", "a170", "a171", "a172", "a173", "a174", "a175", "a176", "a177", "a178", "a179", "a180", "a181", "a182", "a183", "a184", "a185", "a186", "a187", "a188", "a189", "a190", "a191", "a192", "a193", "a194", "a195", "a196", "a197", "a198", "a199", "a200", "a201", "a202", "a203", "a204", "a205", "a206", "a207", "a208", "a209", "a210", "a211", "a212", "a213", "a214", "a215", "a216", "a217", "a218", "a219", "a220", "a221", "a222", "a223", "a224", "a225", "a226", "a227", "a228", "a229", "a230", "a231", "a232", "a233", "a234", "a235", "a236", "a237", "a238", "a239", "a240", "a241", "a242", "a243", "a244", "a245", "a246", "a247", "a248", "a249", "a250", "a251", "a252", "a253", "a254", "a255", "s2", "s3", "s8", "s9", "s10", "s11", "s12", "s13", "s14", "s15", "s16", "s17", "s18", "s19", "s20", "s21", "s22", "s23", "s24", "s25", "s26", "s27", "s28", "s29", "s30", "s31", "s35", "s36", "s37", "s38", "s39", "s40", "s41", "s42", "s43", "s44", "s45", "s46", "s47", "s48", "s49", "s50", "s51", "s52", "s53", "s54", "s55", "s56", "s57", "s58", "s59", "s60", "s61", "s62", "s63", "s64", "s65", "s66", "s67", "vcc", "scc", "memory");
}

extern "C" void kernel_launch(void* const* d_in, const int* in_sizes, int n_in, void* d_out, int out_size,
                              void* d_ws, size_t ws_size, hipStream_t stream) {
  (void)hipFuncSetAttribute((const void*)swin_main, hipFuncAttributeMaxDynamicSharedMemorySize, LDS_BYTES);
  swin_main<<<256, 256, LDS_BYTES, stream>>>(d_in[0], d_in[1], d_in[2], d_ws, d_out);
}
```
